```python
import torch

def _softmax(x, axis=-1):
    return torch.softmax(x, dim=axis)
def _gelu(x):
    return torch.nn.functional.gelu(x, approximate="tanh")
def _silu(x):
    return torch.nn.functional.silu(x)
def _sigmoid(x):
    return torch.sigmoid(x)
def _layernorm(x, axis=-1, eps=1e-5):
    m = x.mean(dim=axis, keepdim=True)
    v = x.var(dim=axis, keepdim=True, unbiased=False)
    return (x - m) / torch.sqrt(v + eps)
def _scatter_add(tgt, idx, src, axis=0):
    return tgt.clone().index_add_(axis, idx, src)
def _segment_sum(src, seg, n):
    out = torch.zeros((n,) + src.shape[1:], dtype=src.dtype)
    return out.index_add_(0, seg, src)
def _pad(x, pw):
    flat = tuple(v for pair in reversed(pw) for v in pair)
    return torch.nn.functional.pad(x, flat)

kernel_name = 'parallel_sum__ssm_conv_s4d__glu_gated_reglu__prompt'

def setup_inputs():

    _w = (lambda a: (lambda t: t.to(torch.bfloat16) if t.is_floating_point() else t)(torch.as_tensor(a)))
    def _A_setup():
        Bsz, L, D, N = 1, 2048, 512, 128
        return {
            "u": _w((lambda *s: torch.randn(*s, dtype=torch.bfloat16) * 0.05)(Bsz, L, D)),
            "log_A_real": _w((lambda *s: torch.randn(*s, dtype=torch.bfloat16) * 3.2)(D, N) * 0.5),
            "A_imag": _w((lambda *s: torch.randn(*s, dtype=torch.bfloat16) * 0.05)(D, N) * 3.14),
            "C_real": _w((lambda *s: torch.randn(*s, dtype=torch.bfloat16) * 0.1)(D, N) / N ** 0.5),
            "C_imag": _w((lambda *s: torch.randn(*s, dtype=torch.bfloat16) * 0.2)(D, N) / N ** 0.5),
            "log_dt": _w((lambda *s: torch.randn(*s, dtype=torch.bfloat16) * 0.05)(D) * 0.5 - 4.0),
            "D_skip": _w((lambda *s: torch.randn(*s, dtype=torch.bfloat16) * 0.05)(D)),
        }
    _A_in = _A_setup()

    def _B_setup():
        B, S, D, F = 1, 2048, 512, 1376
        return {
            "x": _w((lambda *s: torch.randn(*s, dtype=torch.bfloat16) * 0.05)(B, S, D)),
            "W_gate": _w((lambda *s: torch.randn(*s, dtype=torch.bfloat16) * 0.4)(D, F) * (2.0 / D) ** 0.5),
            "W_up": _w((lambda *s: torch.randn(*s, dtype=torch.bfloat16) * 0.4)(D, F) * (2.0 / D) ** 0.5),
            "W_down": _w((lambda *s: torch.randn(*s, dtype=torch.bfloat16) * 0.4)(F, D) * (1.0 / F) ** 0.5),
        }
    _B_in = _B_setup()

    return {
        "A_u": _A_in["u"],
        "A_log_A_real": _A_in["log_A_real"],
        "A_A_imag": _A_in["A_imag"],
        "A_C_real": _A_in["C_real"],
        "A_C_imag": _A_in["C_imag"],
        "A_log_dt": _A_in["log_dt"],
        "A_D_skip": _A_in["D_skip"],
        "B_x": _B_in["x"],
        "B_W_gate": _B_in["W_gate"],
        "B_W_up": _B_in["W_up"],
        "B_W_down": _B_in["W_down"],
    }

def reference(A_u, A_log_A_real, A_A_imag, A_C_real, A_C_imag, A_log_dt, A_D_skip, B_x, B_W_gate, B_W_up, B_W_down):
    def _A_ref(u, log_A_real, A_imag, C_real, C_imag, log_dt, D_skip):
        L = u.shape[1]
        dt = torch.exp(log_dt)
        A = -torch.exp(log_A_real) + 1j * A_imag
        C = (C_real + 1j * C_imag) * dt[:, None]
        pos = (lambda x, dt: x.to(dt))(torch.arange(L), log_dt.dtype)
        P = torch.exp((A * dt[:, None])[:, :, None] * pos[None, None, :])
        K = 2.0 * torch.einsum("dn,dnl->dl", C, P).real
        L2 = 2 * L
        u_t = (lambda x, *d: x.permute(*d))(u, 0, 2, 1)
        Kf = (lambda x, *a, **k: torch.fft.rfft(x.float(), *a, **k))(K, L2)
        Uf = (lambda x, *a, **k: torch.fft.rfft(x.float(), *a, **k))(u_t, L2)
        y = (lambda x, *a, **k: torch.fft.irfft(x, *a, **k).to(torch.bfloat16))(Uf * Kf[None, :, :], L2)[:, :, :L]
        y = (lambda x, *d: x.permute(*d))(y, 0, 2, 1)
        return y + u * D_skip[None, None, :]

    def _B_ref(x, W_gate, W_up, W_down):
        gate = torch.einsum("bsd,df->bsf", x, W_gate)
        up = torch.einsum("bsd,df->bsf", x, W_up)
        gate = gate * (lambda x, dt: x.to(dt))(gate > 0, gate.dtype)
        h = gate * up
        return torch.einsum("bsf,fd->bsd", h, W_down)

    x = A_u
    xn = _layernorm(x, axis=-1)
    return x + _A_ref(xn, A_log_A_real, A_A_imag, A_C_real, A_C_imag, A_log_dt, A_D_skip) + _B_ref(xn, B_W_gate, B_W_up, B_W_down)
```

```cpp
#include <hip/hip_runtime.h>
#include <hip/hip_bf16.h>
#include <stdint.h>

constexpr int L = 2048, D = 512, N = 128, F = 1376;

__device__ __forceinline__ float bf2f(unsigned short b) { return __uint_as_float(((unsigned)b) << 16); }
__device__ __forceinline__ unsigned short f2bf(float f) {
    __hip_bfloat16 h = __float2bfloat16(f);
    return *reinterpret_cast<unsigned short*>(&h);
}
__device__ __forceinline__ float rbf(float f) { return bf2f(f2bf(f)); }

__global__ void k_ln(const unsigned short* x, unsigned short* xn) {
    int row = blockIdx.x, t = threadIdx.x;
    float v[8]; float s = 0;
    for (int j = 0; j < 8; ++j) { v[j] = bf2f(x[row * D + t * 8 + j]); s += v[j]; }
    for (int o = 32; o; o >>= 1) s += __shfl_xor(s, o);
    float m = s / D; float q = 0;
    for (int j = 0; j < 8; ++j) { float d = v[j] - m; q += d * d; }
    for (int o = 32; o; o >>= 1) q += __shfl_xor(q, o);
    float var = rbf(q / D); float mb = rbf(m);
    float sd = rbf(sqrtf(rbf(var + 1e-5f)));
    for (int j = 0; j < 8; ++j) xn[row * D + t * 8 + j] = f2bf(rbf(v[j] - mb) / sd);
}

__global__ void k_kgen(const unsigned short* lAr, const unsigned short* Ai, const unsigned short* Cr,
                       const unsigned short* Ci, const unsigned short* ldt, float* K) {
    int d = blockIdx.y; int l = blockIdx.x * blockDim.x + threadIdx.x;
    float dt = rbf(expf(bf2f(ldt[d])));
    float pos = rbf((float)l);
    float acc = 0;
    for (int n = 0; n < N; ++n) {
        float ar = -rbf(expf(bf2f(lAr[d * N + n]))) * dt;
        float ai = bf2f(Ai[d * N + n]) * dt;
        float cr = bf2f(Cr[d * N + n]) * dt, ci = bf2f(Ci[d * N + n]) * dt;
        float e = expf(ar * pos); float s, c; sincosf(ai * pos, &s, &c);
        acc += e * (cr * c - ci * s);
    }
    K[d * L + l] = 2.f * acc;
}

__global__ void k_gemm1(const unsigned short* xn, const unsigned short* Wg, const unsigned short* Wu, unsigned short* h) {
    int f = blockIdx.x * blockDim.x + threadIdx.x; int l = blockIdx.y;
    if (f >= F) return;
    float g = 0, u = 0;
    for (int k = 0; k < D; ++k) { float a = bf2f(xn[l * D + k]); g += a * bf2f(Wg[k * F + f]); u += a * bf2f(Wu[k * F + f]); }
    g = rbf(g); u = rbf(u);
    g = g > 0 ? g : 0.f * g;
    h[l * F + f] = f2bf(g * u);
}

__global__ void k_out(const unsigned short* x, const unsigned short* xn, const float* K, const unsigned short* Dsk,
                      const unsigned short* h, const unsigned short* Wd, unsigned short* out) {
    int d = threadIdx.x + blockIdx.x * blockDim.x; int l = blockIdx.y;
    float y = 0;
    for (int j = 0; j <= l; ++j) y += K[d * L + j] * bf2f(xn[(l - j) * D + d]);
    float b = 0;
    for (int f = 0; f < F; ++f) b += bf2f(h[l * F + f]) * bf2f(Wd[f * D + d]);
    y = rbf(y); b = rbf(b);
    float a = rbf(y + rbf(bf2f(xn[l * D + d]) * bf2f(Dsk[d])));
    out[l * D + d] = f2bf(rbf(bf2f(x[l * D + d]) + a) + b);
}

extern "C" void kernel_launch(void* const* d_in, const int* in_sizes, int n_in, void* d_out, int out_size, void* d_ws,
                              size_t ws_size, hipStream_t stream) {
    const unsigned short* x = (const unsigned short*)d_in[0];
    const unsigned short* lAr = (const unsigned short*)d_in[1];
    const unsigned short* Ai = (const unsigned short*)d_in[2];
    const unsigned short* Cr = (const unsigned short*)d_in[3];
    const unsigned short* Ci = (const unsigned short*)d_in[4];
    const unsigned short* ldt = (const unsigned short*)d_in[5];
    const unsigned short* Dsk = (const unsigned short*)d_in[6];
    const unsigned short* Wg = (const unsigned short*)d_in[8];
    const unsigned short* Wu = (const unsigned short*)d_in[9];
    const unsigned short* Wd = (const unsigned short*)d_in[10];
    char* ws = (char*)d_ws;
    unsigned short* xn = (unsigned short*)ws;
    float* K = (float*)(ws + (4 << 20));
    unsigned short* h = (unsigned short*)(ws + (16 << 20));
    k_ln<<<L, 64, 0, stream>>>(x, xn);
    k_kgen<<<dim3(L / 256, D), 256, 0, stream>>>(lAr, Ai, Cr, Ci, ldt, K);
    k_gemm1<<<dim3((F + 255) / 256, L), 256, 0, stream>>>(xn, Wg, Wu, h);
    k_out<<<dim3(D / 256, L), 256, 0, stream>>>(x, xn, K, Dsk, h, Wd, (unsigned short*)d_out);
}
```

```cpp
#include <hip/hip_runtime.h>
#include <stdint.h>

namespace {

typedef unsigned short u16;
typedef unsigned v4u __attribute__((ext_vector_type(4)));
typedef unsigned v2u __attribute__((ext_vector_type(2)));
typedef short s16x4 __attribute__((ext_vector_type(4)));
typedef __bf16 bf16x8 __attribute__((ext_vector_type(8)));
typedef __bf16 bf16x2 __attribute__((ext_vector_type(2)));
typedef _Float16 f16x8 __attribute__((ext_vector_type(8)));
typedef _Float16 f16x2 __attribute__((ext_vector_type(2)));
typedef float f32x16 __attribute__((ext_vector_type(16)));
typedef float f32x4 __attribute__((ext_vector_type(4)));
typedef float f32x2 __attribute__((ext_vector_type(2)));
#define LAS __attribute__((address_space(3)))
#define GAS __attribute__((address_space(1)))
#define DEVI __device__ __forceinline__
#define SB() __builtin_amdgcn_sched_barrier(0)

constexpr int L_ = 2048, D_ = 512, N_ = 128, F_ = 1376;
constexpr int KS2 = F_ / 16;

constexpr size_t OFF_HT = 0;
constexpr size_t OFF_WDT = (size_t)6 << 20;
constexpr size_t OFF_XT = (size_t)8 << 20;
constexpr size_t OFF_KG = (size_t)10 << 20;
constexpr size_t OFF_ST = (size_t)12 << 20;
constexpr size_t OFF_STATS = (size_t)14 << 20;
constexpr size_t OFF_ISC = OFF_STATS + 16384;
constexpr int WS_BYTES = 15 << 20;
constexpr unsigned SENT = 0xFFFFFFFFu;
#define AUX_EARLY 16
#define AUX_H 16

#define STAMP_DECL
#define STAMP_ARG
#define STAMP_PASS
#define STAMP(idx) do {} while (0)
#define STAMP_DUMP(kern) do {} while (0)
#define CSTAMP(idx) do {} while (0)

DEVI float bf_lo(unsigned u) { return __builtin_bit_cast(float, u << 16); }
DEVI float bf_hi(unsigned u) { return __builtin_bit_cast(float, u & 0xFFFF0000u); }
DEVI unsigned pk_bf(float a, float b) { bf16x2 v = {(__bf16)a, (__bf16)b}; return __builtin_bit_cast(unsigned, v); }
DEVI unsigned pk_h(float a, float b) { f16x2 v = {(_Float16)a, (_Float16)b}; return __builtin_bit_cast(unsigned, v); }
DEVI float relu(float a) { return __builtin_bit_cast(float, max(__builtin_bit_cast(int, a), 0)); }
DEVI float rbf(float a) { return bf_lo(pk_bf(a, a)); }
DEVI float dot2bf(unsigned a, unsigned b, float c) {
  return __builtin_amdgcn_fdot2_f32_bf16(__builtin_bit_cast(bf16x2, a), __builtin_bit_cast(bf16x2, b), c, false);
}
DEVI f32x16 mfma32(v4u a, v4u b, f32x16 c) {
  return __builtin_amdgcn_mfma_f32_32x32x16_bf16(__builtin_bit_cast(bf16x8, a), __builtin_bit_cast(bf16x8, b), c, 0, 0, 0);
}
DEVI f32x4 mfma16b(v4u a, v4u b, f32x4 c) {
  return __builtin_amdgcn_mfma_f32_16x16x32_bf16(__builtin_bit_cast(bf16x8, a), __builtin_bit_cast(bf16x8, b), c, 0, 0, 0);
}
DEVI f32x4 mfma16h(v4u a, v4u b, f32x4 c) {
  return __builtin_amdgcn_mfma_f32_16x16x32_f16(__builtin_bit_cast(f16x8, a), __builtin_bit_cast(f16x8, b), c, 0, 0, 0);
}
DEVI v2u tr_read(LAS char* p) { return __builtin_bit_cast(v2u, __builtin_amdgcn_ds_read_tr16_b64_v4i16((LAS s16x4*)p)); }
DEVI v4u tr_read2(LAS char* p0, LAS char* p1) { v2u a = tr_read(p0), b = tr_read(p1); return v4u{a.x, a.y, b.x, b.y}; }
DEVI void glds16(const void* g, LAS char* l) { __builtin_amdgcn_global_load_lds((const GAS unsigned*)g, (LAS unsigned*)l, 16, 0, 0); }
#define WG_BARRIER() asm volatile("s_waitcnt lgkmcnt(0)\n\ts_barrier" ::: "memory")
template <int N> DEVI void wait_vm() { asm volatile("s_waitcnt vmcnt(%0)" ::"n"(N) : "memory"); }
typedef __amdgpu_buffer_rsrc_t rsrc_t;
DEVI rsrc_t ws_rsrc(char* ws) { return __builtin_amdgcn_make_buffer_rsrc(ws, 0, WS_BYTES, 0x00020000); }
template <int AUX> DEVI void st128(rsrc_t rs, int off, v4u v) { __builtin_amdgcn_raw_buffer_store_b128(v, rs, off, 0, AUX); }

DEVI void lds_arrive(LAS unsigned* cnt) {
  asm volatile("s_waitcnt lgkmcnt(0)" ::: "memory");
  if ((threadIdx.x & 63) == 0) __hip_atomic_fetch_add(cnt, 1u, __ATOMIC_RELAXED, __HIP_MEMORY_SCOPE_WORKGROUP);
}
DEVI void lds_wait(LAS unsigned* cnt, unsigned target) {
  while (__builtin_amdgcn_readfirstlane(__hip_atomic_load(cnt, __ATOMIC_RELAXED, __HIP_MEMORY_SCOPE_WORKGROUP)) < target) {}
  asm volatile("" ::: "memory");
}

DEVI f32x2 cmul(f32x2 a, f32x2 w, f32x2 wr) { return __builtin_elementwise_fma(a.yy, wr, a.xx * w); }
DEVI f32x2 rot90(f32x2 w) { return f32x2{-w.y, w.x}; }

constexpr int SLOT = 40960;
constexpr int K1_SCR = 3 * SLOT;
constexpr int KG_QS = 528, KG_WAVE = 16 * KG_QS + 256;
constexpr int K1_WD = K1_SCR + 4096;
constexpr int K1_CS = K1_SCR + 6144;
constexpr int K1_LDS = K1_SCR + 4 * KG_WAVE;

struct K1Args {
  const u16 *x, *lAr, *Ai, *Cr, *Ci, *ldt, *Wg, *Wu, *Wd;
  char* ws;
};

template <int NU>
DEVI void k1_loader(const K1Args& a, LAS char* sm, int lw, int lane, int it, int u0, int rot STAMP_ARG) {
  constexpr int RS = 64 * NU, NI = 8 + 4 * NU;
  unsigned xo[2], wo[2 * NU];
#pragma unroll
  for (int i = 0; i < 2; ++i) {
    const int row = lw * 64 + i * 8 + (lane >> 3);
    xo[i] = (unsigned)((it * 128 + row) * D_ + (((lane & 7) ^ ((row >> 1) & 7)) << 3)) * 2u;
  }
#pragma unroll
  for (int i = 0; i < 2 * NU; ++i) {
    const int e = i * 64 + lane, row = e / (4 * NU), c = e % (4 * NU);
    wo[i] = (unsigned)((lw * 32 + row) * F_ + u0 * 32 + c * 8) * 2u;
  }
  auto issue = [&](int cc) {
    LAS char* base = sm + (cc % 3) * SLOT;
    const int c = (cc + rot) & 7;
    const char* xs = (const char*)a.x + c * 128;
    const char* g = (const char*)a.Wg + c * (64 * F_ * 2);
    const char* u = (const char*)a.Wu + c * (64 * F_ * 2);
#pragma unroll
    for (int i = 0; i < 8; ++i) glds16(xs + (i >> 1) * (16 * D_ * 2) + xo[i & 1], base + (lw * 8 + i) * 1024);
#pragma unroll
    for (int i = 0; i < 2 * NU; ++i) glds16(g + wo[i], base + 16384 + lw * 32 * RS + i * 1024);
#pragma unroll
    for (int i = 0; i < 2 * NU; ++i) glds16(u + wo[i], base + 16384 + 64 * RS + lw * 32 * RS + i * 1024);
  };
  issue(0); issue(1);
  STAMP(1);
  wait_vm<NI>(); STAMP(2); WG_BARRIER();
  STAMP(3);
  issue(2);
  wait_vm<NI>(); WG_BARRIER();
  {
#pragma unroll
    for (int v = 0; v < 2; ++v) {
      const int bi = min(lw + 2 * v, 2), blk = min((int)blockIdx.x + 256 * bi, 687);
      const char* src = (const char*)a.Wd + ((blk >> 3) * 16 * D_ + (blk & 7) * 64) * 2;
#pragma unroll
      for (int i = 0; i < 2; ++i)
        glds16(src + (unsigned)(((i * 8 + (lane >> 3)) * D_ + (lane & 7) * 8) * 2), sm + K1_WD + bi * KG_WAVE + i * 1024);
    }
  }
  issue(3);
  wait_vm<NI + 4>(); WG_BARRIER();
  issue(4);
  wait_vm<NI>(); WG_BARRIER();
  issue(5);
  wait_vm<NI>(); WG_BARRIER();
  issue(6);
  wait_vm<NI>(); WG_BARRIER();
  issue(7);
  wait_vm<NI>(); WG_BARRIER();
  wait_vm<0>(); STAMP(4); WG_BARRIER();
  STAMP(5);
}

DEVI void k1_misc(const K1Args& a, LAS char* sm, int m, int lane, int it, int ft, int rot STAMP_ARG) {
  const rsrc_t rs = ws_rsrc(a.ws);
  const int b = blockIdx.x, G = lane >> 4, q = (lane >> 2) & 3, p = lane & 3, hh = lane >> 5;
#pragma unroll
  for (int i = 0; i < 4; ++i) st128<AUX_EARLY>(rs, (int)OFF_ST + b * 8192 + (m * 4 + i) * 1024 + lane * 16, v4u{SENT, SENT, SENT, SENT});
  const int cstar = ((ft >> 1) - rot) & 7;
  int slot = 0;
#pragma unroll 1
  for (int cc = 0; cc < 8; ++cc) {
    WG_BARRIER();
    if (cc == cstar) {
      LAS char* base = sm + slot * SLOT;
#pragma unroll
      for (int i = 0; i < 4; ++i) {
        const int cb = i & 1, rb = m * 64 + (i >> 1) * 32 + G * 8, r0 = rb + q, r1 = r0 + 4, sl = (ft & 1) * 4 + cb * 2 + (p >> 1);
        const v4u v = tr_read2(base + r0 * 128 + ((sl ^ ((r0 >> 1) & 7)) << 4) + (p & 1) * 8,
                               base + r1 * 128 + ((sl ^ ((r1 >> 1) & 7)) << 4) + (p & 1) * 8);
        st128<AUX_EARLY>(rs, (int)OFF_XT + (ft * 32 + cb * 16 + (lane & 15)) * 4096 + (it * 128 + rb) * 2, v);
      }
    }
    if (cc == 0) {
      LAS float* t0 = (LAS float*)(sm + K1_SCR + 2 * m * KG_WAVE);
      constexpr int T1 = KG_WAVE / 4;
      const int ko = (int)OFF_KG + (2 * b + m) * 4096 + lane * 16;
#pragma unroll
      for (int c = 0; c < 2; ++c) {
        const int M = 255 - 64 * c - lane;
        const float lo = t0[384 + M] + t0[T1 + 384 + M], hi = t0[385 + M] + t0[T1 + 385 + M];
        const unsigned ll = pk_h(lo, lo);
        st128<AUX_EARLY>(rs, ko + c * 1024, v4u{pk_h(hi, hi), pk_h(hi, (M & 1) ? hi : lo), ll, ll});
      }
      {
        const int i0 = 256 + 2 * (127 - lane);
        const float e0 = t0[i0] + t0[T1 + i0], e1 = t0[i0 + 1] + t0[T1 + i0 + 1], e2 = t0[i0 + 2] + t0[T1 + i0 + 2];
        st128<AUX_EARLY>(rs, ko + 2048, v4u{pk_h(e2, e2), pk_h(e1, e1), pk_h(e1, e0), pk_h(e0, e0)});
      }
      {
        const int M = 63 - lane;
        const bool r1 = M >= 32;
        LAS f32x4* tp = (LAS f32x4*)(t0 + (r1 ? 128 + 4 * M : 8 * M));
        const f32x4 va = tp[0] + tp[T1 / 4], vb = tp[1] + tp[T1 / 4 + 1];
        st128<AUX_EARLY>(rs, ko + 3072,
                         v4u{pk_h(r1 ? vb[0] : vb[3], r1 ? va[3] : vb[2]), pk_h(r1 ? va[2] : vb[1], r1 ? va[2] : vb[0]),
                             pk_h(r1 ? va[2] : va[3], r1 ? va[1] : va[2]), pk_h(r1 ? va[0] : va[1], va[0])});
      }
      if (lane == 0) ((float*)(a.ws + OFF_ISC))[2 * b + m] = t0[768];
    }
    if (cc == 4) {
#pragma unroll
      for (int v = 0; v < 2; ++v) {
        const int bi = m + 2 * v, blk = b + 256 * bi;
        if (blk < 688) {
          LAS char* src = sm + K1_WD + bi * KG_WAVE + (4 * hh + q) * 128 + (G & 1) * 32 + p * 8;
#pragma unroll
          for (int t = 0; t < 2; ++t)
            st128<AUX_EARLY>(rs, (int)OFF_WDT + (((blk & 7) * 2 + t) * KS2 + (blk >> 3)) * 1024 + lane * 16,
                             tr_read2(src + t * 64, src + t * 64 + 8 * 128));
        }
      }
    }
    slot = slot == 2 ? 0 : slot + 1;
  }
  STAMP(5);
}

DEVI void k1_kgen(LAS char* sm, int w, int lane, unsigned lar, unsigned ai, unsigned cr, unsigned ci, unsigned cr2, unsigned ci2, unsigned ld STAMP_ARG) {
  const float LOG2E = 1.4426950408889634f;
  const float dt = rbf(__builtin_amdgcn_exp2f(bf_lo(ld) * LOG2E));
  const float a2 = -rbf(__builtin_amdgcn_exp2f(bf_lo(lar) * LOG2E)) * dt * LOG2E;
  const float bb = bf_lo(ai) * dt;
  const float I2PH = 0.15915494f, I2PL = 6.4206382e-09f;
  const float bh = bb * I2PH, bl = __builtin_fmaf(bb, I2PL, __builtin_fmaf(bb, I2PH, -bh));
  auto zpow = [&](float p) {
    const float mag = __builtin_amdgcn_exp2f(a2 * p);
    const float th = __builtin_fmaf(bl, p, __builtin_amdgcn_fractf(bh * p));
    return f32x2{mag * __builtin_amdgcn_cosf(th), mag * __builtin_amdgcn_sinf(th)};
  };
  unsigned mx = max(max(cr & 0x7FFFu, ci & 0x7FFFu), max(cr2 & 0x7FFFu, ci2 & 0x7FFFu));
  mx = max(mx, (unsigned)__builtin_amdgcn_update_dpp(0, (int)mx, 0x121, 0xF, 0xF, false));
  mx = max(mx, (unsigned)__builtin_amdgcn_update_dpp(0, (int)mx, 0x122, 0xF, 0xF, false));
  mx = max(mx, (unsigned)__builtin_amdgcn_update_dpp(0, (int)mx, 0x124, 0xF, 0xF, false));
  mx = max(mx, (unsigned)__builtin_amdgcn_update_dpp(0, (int)mx, 0x128, 0xF, 0xF, false));
  unsigned E = max(max((unsigned)__builtin_amdgcn_readlane(mx, 0), (unsigned)__builtin_amdgcn_readlane(mx, 16)),
                   max((unsigned)__builtin_amdgcn_readlane(mx, 32), (unsigned)__builtin_amdgcn_readlane(mx, 48))) >> 7;
  E = min(max(E, 8u), 254u);
  const float sc = __builtin_bit_cast(float, (260u - E) << 23);
  const float isc = 2.0f * dt * __builtin_bit_cast(float, (E - 6u) << 23);

  LAS char* b0 = sm + K1_SCR + w * KG_WAVE;
  LAS unsigned* wp = (LAS unsigned*)(b0 + (lane >> 2) * KG_QS + (lane & 3) * 4);
  auto put = [&](int row, f32x2 v) { wp[row * 4] = pk_h(v.x, v.y); };
  LAS char* fp = b0 + (lane >> 4) * KG_QS + (lane & 15) * 16;
  auto frag = [&](int tile, int s) { return *(LAS v4u*)(fp + s * 4 * KG_QS + tile * 256); };

  const f32x2 z8 = zpow(8.f), z8r = rot90(z8);
  f32x2 z1 = zpow(1.f), zw = zpow(128.f);
  z1.y = -z1.y; zw.y = -zw.y;
  const f32x2 z1r = rot90(z1), zwr = rot90(zw);
  f32x2 g = {bf_lo(cr) * sc, -bf_lo(ci) * sc};
  const f32x2 g1 = cmul(g, zw, zwr);
  const f32x2 z16 = cmul(z8, z8, z8r), z16r = rot90(z16);
  STAMP(2);
  {
    f32x2 p = g, q = g1, y0 = {1.f, 0.f}, y1 = z8;
#pragma unroll
    for (int i = 0; i < 8; ++i) {
      put(2 * i, y0); put(2 * i + 1, y1);
      put(16 + i, p); put(24 + i, q);
      if (i < 7) { y0 = i ? cmul(y0, z16, z16r) : z16; y1 = cmul(y1, z16, z16r); p = cmul(p, z1, z1r); q = cmul(q, z1, z1r); }
    }
  }
  v4u yb[4], xa[4];
#pragma unroll
  for (int s = 0; s < 4; ++s) { yb[s] = frag(0, s); xa[s] = frag(1, s); }
  SB();
  const f32x2 z2 = cmul(z1, z1, z1r), z2r = rot90(z2);
  const f32x2 z4 = cmul(z2, z2, z2r), z4r = rot90(z4);
  const f32x2 zw2 = cmul(zw, zw, zwr), zw2r = rot90(zw2);
  f32x2 ge = cmul(g, zw2, zw2r), go = cmul(g1, zw2, zw2r);
  {
    f32x2 p = ge, q = go;
#pragma unroll
    for (int i = 0; i < 4; ++i) { put(i, p); put(4 + i, q); if (i < 3) { p = cmul(p, z2, z2r); q = cmul(q, z2, z2r); } }
  }
#pragma unroll
  for (int aa = 4; aa < 8; aa += 2) {
    ge = cmul(ge, zw2, zw2r); go = cmul(go, zw2, zw2r);
    put(8 + (aa - 4) * 2, ge); put(9 + (aa - 4) * 2, cmul(ge, z4, z4r));
    put(10 + (aa - 4) * 2, go); put(11 + (aa - 4) * 2, cmul(go, z4, z4r));
  }
#pragma unroll
  for (int aa = 8; aa <= 16; aa += 2) {
    ge = cmul(ge, zw2, zw2r); put(16 + aa - 8, ge);
    if (aa < 16) { go = cmul(go, zw2, zw2r); put(17 + aa - 8, go); }
  }
  v4u xb[4], xc[4];
#pragma unroll
  for (int s = 0; s < 4; ++s) { xb[s] = frag(0, s); xc[s] = frag(1, s); }
  SB();
  f32x4 d0 = {}, d1 = {}, d2 = {};
#pragma unroll
  for (int s = 0; s < 4; ++s) d0 = mfma16h(xa[s], yb[s], d0);
#pragma unroll
  for (int s = 0; s < 4; ++s) { d1 = mfma16h(xb[s], yb[s], d1); d2 = mfma16h(xc[s], yb[s], d2); }
  LAS float* tab = (LAS float*)b0;
  const int j = lane & 15, gq = lane >> 4;
  *(LAS f32x4*)(tab + 128 * (gq >> 1) + 4 * (gq & 1) + 8 * j) = d0;
  {
    const int i0 = gq < 2 ? 256 + 64 * gq + 4 * j : 384 + 64 * (gq - 2) + 2 * j, st = gq < 2 ? 2 : 32;
    *(LAS f32x2*)(tab + i0) = f32x2{d1[0], d1[1]};
    *(LAS f32x2*)(tab + i0 + st) = f32x2{d1[2], d1[3]};
  }
#pragma unroll
  for (int i = 0; i < 4; ++i) tab[512 + 64 * gq + 16 * i + j] = d2[i];
  tab[768] = isc;
}

template <int NU> struct Frags { v4u x, w[2 * NU]; };

template <int NU>
DEVI void k1_consumer(const K1Args& a, LAS char* sm, int w, int lane, int it, int ft, int u0 STAMP_ARG) {
  constexpr int RS = 64 * NU, NT = 2 * NU;
  const int ch = 2 * blockIdx.x + (w >> 1), n = ch * N_ + 64 * (w & 1) + lane;
  const unsigned lar = a.lAr[n], ai = a.Ai[n], cr = a.Cr[n], ci = a.Ci[n], cr2 = a.Cr[n ^ 64], ci2 = a.Ci[n ^ 64], ld = a.ldt[ch];
  SB();
  const int r32 = lane & 31, hh = lane >> 5, G = lane >> 4, q = (lane >> 2) & 3, p = lane & 3;
  int xo[4], wo[4];
#pragma unroll
  for (int kk = 0; kk < 4; ++kk) {
    const int ks = (kk + w) & 3;
    xo[kk] = (w * 32 + r32) * 128 + (((2 * ks + hh) ^ ((r32 >> 1) & 7)) << 4);
    wo[kk] = 16384 + (16 * ks + 8 * hh + q) * RS + (G & 1) * 32 + p * 8;
    asm volatile("" : "+v"(xo[kk]), "+v"(wo[kk]));
  }
  f32x16 acc[NT];
  v4u sel[NT + 1];
  Frags<NU> F0, F1;
#pragma unroll
  for (int t = 0; t < NT; ++t) { acc[t] = f32x16{}; asm volatile("" : "+v"(acc[t])); F1.w[t] = v4u{}; asm volatile("" : "+v"(F1.w[t])); }
#pragma unroll
  for (int t = 0; t <= NT; ++t) {
    const unsigned s = (lane & 15) == 2 * t + (G & 1) ? 0x3F803F80u : 0u;
    sel[t] = v4u{s, s, s, s};
    asm volatile("" : "+v"(sel[t]));
  }
  F1.x = v4u{};
  asm volatile("" : "+v"(F1.x));
  f32x4 sums = {};
  float s2a = 0.f, s2b = 0.f;
  SB();
  STAMP(1);
  k1_kgen(sm, w, lane, lar, ai, cr, ci, cr2, ci2, ld STAMP_PASS);
  STAMP(3);

  auto rdw = [&](LAS char* wp, int t) { LAS char* s = wp + (t & 1) * 64 * RS + (t >> 1) * 64; return tr_read2(s, s + 4 * RS); };
  auto step = [&](Frags<NU>& Fn, LAS char* base, int kn, const Frags<NU>& Fc, int kc, bool rd) {
    LAS char* wp = base + wo[kn];
    if (rd) { Fn.x = *(LAS v4u*)(base + xo[kn]); Fn.w[0] = rdw(wp, 0); Fn.w[1] = rdw(wp, 1); }
    SB();
    sums = mfma16b(sel[NT], Fc.x, sums);
    acc[0] = mfma32(Fc.w[0], Fc.x, acc[0]);
    SB();
    if (rd) { Fn.w[2] = rdw(wp, 2); Fn.w[3] = rdw(wp, 3); }
    SB();
    sums = mfma16b(sel[kc], Fc.w[kc], sums);
    acc[1] = mfma32(Fc.w[1], Fc.x, acc[1]);
    SB();
    if (NU == 3) {
      if (rd) { Fn.w[NT - 2] = rdw(wp, NT - 2); Fn.w[NT - 1] = rdw(wp, NT - 1); }
      SB();
      if (kc < 2) sums = mfma16b(sel[NT - 2 + (kc & 1)], Fc.w[NT - 2 + (kc & 1)], sums);
    }
#pragma unroll
    for (int t = 2; t < NT; ++t) acc[t] = mfma32(Fc.w[t], Fc.x, acc[t]);
    if (NU == 2) {
      s2a = dot2bf(Fc.x[0], Fc.x[0], s2a); s2b = dot2bf(Fc.x[1], Fc.x[1], s2b);
      s2a = dot2bf(Fc.x[2], Fc.x[2], s2a); s2b = dot2bf(Fc.x[3], Fc.x[3], s2b);
    }
    SB();
  };
  int slot = 0;
#pragma unroll 1
  for (int cc = 0; cc < 8; ++cc) {
    WG_BARRIER();
    LAS char* base = sm + slot * SLOT;
    step(F0, base, 0, F1, 3, true);
    step(F1, base, 1, F0, 0, true);
    step(F0, base, 2, F1, 1, true);
    step(F1, base, 3, F0, 2, true);
    slot = slot == 2 ? 0 : slot + 1;
  }
  STAMP(4); CSTAMP(11);
  sums = mfma16b(sel[NT], F1.x, sums);
  sums = mfma16b(sel[3], F1.w[3], sums);
  acc[0] = mfma32(F1.w[0], F1.x, acc[0]);
  SB();
  LAS float* csp = (LAS float*)(sm + K1_CS);
#pragma unroll
  for (int i = 0; i < 4; ++i) csp[w * (KG_WAVE / 4) + (4 * G + i) * 16 + (lane & 15)] = sums[i];
  SB();
  acc[1] = mfma32(F1.w[1], F1.x, acc[1]);
  SB();
  WG_BARRIER();
  float cs[NT];
#pragma unroll
  for (int t = 0; t < NT; ++t) cs[t] = csp[2 * hh * (KG_WAVE / 4) + t * 32 + r32] + csp[(2 * hh + 1) * (KG_WAVE / 4) + t * 32 + r32];
  const float s1 = csp[w * (KG_WAVE / 4) + NT * 32 + r32];
#pragma unroll
  for (int t = 2; t < NT; ++t) acc[t] = mfma32(F1.w[t], F1.x, acc[t]);
  const float mean = s1 * (1.0f / D_);
  const unsigned mb = pk_bf(-mean, -mean);
  const v4u mfr = {mb, mb, mb, mb};
#pragma unroll
  for (int t = 0; t < NT; ++t) {
    const float h1 = rbf(cs[t]), r1 = cs[t] - h1, h2 = rbf(r1);
    acc[t] = mfma32(v4u{pk_bf(h1, h2), pk_bf(r1 - h2, 0.f), 0u, 0u}, mfr, acc[t]);
  }
  const rsrc_t rs = ws_rsrc(a.ws);
  const int ho = (int)OFF_HT + ((it * 4 + w) * KS2 + 2 * u0) * 1024 + lane * 16;
#pragma unroll
  for (int u = 0; u < NU; ++u) {
#pragma unroll
    for (int s = 0; s < 2; ++s) {
      v4u o;
#pragma unroll
      for (int e = 0; e < 4; ++e) {
        const int i = 8 * s + 2 * e;
        o[e] = pk_bf(relu(acc[2 * u][i]) * acc[2 * u + 1][i], relu(acc[2 * u][i + 1]) * acc[2 * u + 1][i + 1]);
      }
      st128<AUX_H>(rs, ho + (2 * u + s) * 1024, o);
    }
  }
  if (NU == 2) {
    s2a = dot2bf(F1.x[0], F1.x[0], s2a); s2b = dot2bf(F1.x[1], F1.x[1], s2b);
    s2a = dot2bf(F1.x[2], F1.x[2], s2a); s2b = dot2bf(F1.x[3], F1.x[3], s2b);
    float s2 = s2a + s2b;
    s2 += __shfl_xor(s2, 32);
    const float var = fmaxf(s2 * (1.0f / D_) - mean * mean, 0.f);
    if (ft == 15 && hh == 0)
      ((unsigned*)(a.ws + OFF_STATS))[it * 128 + w * 32 + r32] = pk_bf(mean, __builtin_sqrtf(rbf(rbf(var) + 1e-5f)));
  }
  STAMP(5);
}

#define ROTMODE 0
__global__ __launch_bounds__(512, 2) void k1(K1Args a) {
  extern __shared__ __attribute__((aligned(16))) char smem[];
  LAS char* sm = (LAS char*)smem;
  char* ws = a.ws;
  STAMP_DECL
  STAMP(0);
  const int tid = threadIdx.x, lane = tid & 63, w = __builtin_amdgcn_readfirstlane(tid >> 6);
  const int b = blockIdx.x, xcd = b & 7, r = b >> 3;
  const int it = (xcd & 1) * 8 + (r & 7), ft = (xcd >> 1) * 4 + (r >> 3);
  const int rot = ROTMODE == 0 ? ((r & 7) + 2 * (r >> 3)) & 7 : ROTMODE == 1 ? (2 * ((r & 3) + (r >> 3))) & 7 : ROTMODE == 2 ? (4 * ((r & 1) + (r >> 3))) & 7 : 0;
  if (w >= 6) k1_misc(a, sm, w - 6, lane, it, ft, rot STAMP_PASS);
  else if (ft < 11) {
    if (w >= 4) k1_loader<3>(a, sm, w - 4, lane, it, ft * 3, rot STAMP_PASS);
    else k1_consumer<3>(a, sm, w, lane, it, ft, ft * 3 STAMP_PASS);
  } else {
    if (w >= 4) k1_loader<2>(a, sm, w - 4, lane, it, 11 + ft * 2, rot STAMP_PASS);
    else k1_consumer<2>(a, sm, w, lane, it, ft, 11 + ft * 2 STAMP_PASS);
  }
  STAMP_DUMP(0);
}

constexpr int SC_STR = 4160;
constexpr int K2_S = 0;
constexpr int U_STR = 4608;
constexpr int K2_U = K2_S + 8 * SC_STR;
constexpr int K2_Y = K2_U + 2 * U_STR;
constexpr int RED_ROW = 272, RED_W = 64 * RED_ROW;
constexpr int K2_RED = K2_Y + 4 * 8192;
constexpr int SR_ROW = 144;
constexpr int K2_SR = K2_RED + 4 * RED_W;
constexpr int K2_FLAG = K2_SR + 64 * SR_ROW;
constexpr int K2_LDS = K2_FLAG + 64;
#define RING 10
#define PRIO_C 2
#define PRIO_G 0
#define K2MAP 1

struct K2Args {
  const u16* Dsk;
  char* ws;
  u16* out;
};

__global__ __launch_bounds__(512, 2) void k2(K2Args a) {
  extern __shared__ __attribute__((aligned(16))) char smem[];
  LAS char* sm = (LAS char*)smem;
  char* ws = a.ws;
  STAMP_DECL
  STAMP(0);
  const int tid = threadIdx.x, lane = tid & 63, w = __builtin_amdgcn_readfirstlane(tid >> 6);
  const int b = blockIdx.x, xcd = b & 7, r = b >> 3;
  const int lt = K2MAP == 2 ? (xcd & 1) * 16 + (r & 15) : K2MAP == 1 ? (xcd & 3) * 8 + (r & 7) : xcd * 4 + (r & 3);
  const int dt = K2MAP == 2 ? (xcd >> 1) * 2 + (r >> 4) : K2MAP == 1 ? (xcd >> 2) * 4 + (r >> 3) : r >> 2;
  LAS unsigned* flag = (LAS unsigned*)(sm + K2_FLAG);
  unsigned stat;
  const rsrc_t rs = ws_rsrc(ws);
  if (w < 4) {
    if (PRIO_G) __builtin_amdgcn_s_setprio(PRIO_G);
    const int ks0 = w * 22 - (w == 3 ? 1 : 0), n = w < 2 ? 22 : 21;
    const int ho = (int)OFF_HT + (lt * 2 * KS2 + ks0) * 1024, wo = (int)OFF_WDT + (dt * 2 * KS2 + ks0) * 1024;
    const int lo = lane * 16;
    v4u f[RING][4];
    auto ld = [&](int i) {
      f[i % RING][0] = __builtin_amdgcn_raw_buffer_load_b128(rs, lo, ho + i * 1024, 0);
      f[i % RING][1] = __builtin_amdgcn_raw_buffer_load_b128(rs, lo, wo + i * 1024, 0);
      f[i % RING][2] = __builtin_amdgcn_raw_buffer_load_b128(rs, lo, ho + (KS2 + i) * 1024, 0);
      f[i % RING][3] = __builtin_amdgcn_raw_buffer_load_b128(rs, lo, wo + (KS2 + i) * 1024, 0);
      SB();
    };
    if (tid == 0) { flag[0] = 0u; flag[1] = 0u; }
    WG_BARRIER();
    stat = __builtin_amdgcn_raw_buffer_load_b32(rs, (lt * 64 + (tid >> 3)) * 4, (int)OFF_STATS, 0);
#pragma unroll
    for (int i = 0; i < RING; ++i) ld(i);
    STAMP(9);
    f32x16 acc[2][2] = {};
#pragma unroll
    for (int i = 0; i < 22; ++i) {
      if (i < 21 || n == 22) {
        const v4u h0 = f[i % RING][0], w0 = f[i % RING][1], h1 = f[i % RING][2], w1 = f[i % RING][3];
        acc[0][0] = mfma32(w0, h0, acc[0][0]);
        acc[0][1] = mfma32(w0, h1, acc[0][1]);
        acc[1][0] = mfma32(w1, h0, acc[1][0]);
        acc[1][1] = mfma32(w1, h1, acc[1][1]);
      }
      SB();
      if (i == 0) { STAMP(3); CSTAMP(10); } if (i == 5) STAMP(4); if (i == 10) STAMP(5); if (i == 15) STAMP(6);
      if (i + RING < 21 || (i + RING == 21 && n == 22)) ld(i + RING);
      SB();
    }
    STAMP(1); CSTAMP(11);
    LAS char* rp = sm + K2_RED + w * RED_W + (lane & 31) * RED_ROW + (lane >> 5) * 16;
#pragma unroll
    for (int T = 0; T < 2; ++T)
#pragma unroll
      for (int Nn = 0; Nn < 2; ++Nn)
#pragma unroll
        for (int qd = 0; qd < 4; ++qd)
          *(LAS f32x4*)(rp + Nn * 32 * RED_ROW + T * 128 + qd * 32) =
              f32x4{acc[T][Nn][4 * qd], acc[T][Nn][4 * qd + 1], acc[T][Nn][4 * qd + 2], acc[T][Nn][4 * qd + 3]};
    STAMP(2);
  } else {
    if (PRIO_C) __builtin_amdgcn_s_setprio(PRIO_C);
    const int cw = w - 4, t = tid - 256;
    v4u xv[2], kv[2]; v2u kp[2];
    const int t16 = t * 16;
#pragma unroll
    for (int c = 0; c < 2; ++c) {
      kv[c] = __builtin_amdgcn_raw_buffer_load_b128(rs, t16, (int)OFF_KG + (2 * b + c) * 4096, 0);
      kp[c] = __builtin_amdgcn_raw_buffer_load_b64(rs, t16, (int)OFF_KG + (2 * b + c) * 4096 - 8, 0);
    }
    const v4u st0 = __builtin_amdgcn_raw_buffer_load_b128(rs, 2 * t16, (int)OFF_STATS, 0), st1 = __builtin_amdgcn_raw_buffer_load_b128(rs, 2 * t16 + 16, (int)OFF_STATS, 0);
#pragma unroll
    for (int c = 0; c < 2; ++c) xv[c] = __builtin_amdgcn_raw_buffer_load_b128(rs, t16, (int)OFF_XT + (2 * b + c) * 4096, 0);
    SB();
    WG_BARRIER();
    stat = __builtin_amdgcn_raw_buffer_load_b32(rs, (lt * 64 + (tid >> 3)) * 4, (int)OFF_STATS, 0);
    const unsigned dsk = *(const unsigned*)(a.Dsk + 2 * b);
    const f32x2 isc = *(const f32x2*)(ws + OFF_ISC + 8 * b);
    if (t < 60) *(LAS v4u*)(sm + K2_U + (t >= 30 ? U_STR - 480 : 0) + t * 16) = v4u{0u, 0u, 0u, 0u};
    if (t >= 64 && t < 80) *(LAS v4u*)(sm + K2_S + ((t - 64) >> 1) * SC_STR + 4112 + (t & 1) * 16) = v4u{0u, 0u, 0u, 0u};
    STAMP(5);
#pragma unroll
    for (int c = 0; c < 2; ++c) {
      if (t == 0) kp[c] = v2u{0u, 0u};
      const v4u k = kv[c]; const v2u pz = kp[c];
      LAS char* s = sm + K2_S + c * 4 * SC_STR + t * 16;
      const unsigned a0 = __builtin_amdgcn_alignbit(pz.y, pz.x, 16), a1 = __builtin_amdgcn_alignbit(k.x, pz.y, 16),
                     a2 = __builtin_amdgcn_alignbit(k.y, k.x, 16), a3 = __builtin_amdgcn_alignbit(k.z, k.y, 16),
                     a4 = __builtin_amdgcn_alignbit(k.w, k.z, 16);
      *(LAS v4u*)(s) = k;
      *(LAS v4u*)(s + SC_STR) = v4u{a1, a2, a3, a4};
      *(LAS v4u*)(s + 2 * SC_STR) = v4u{pz.y, k.x, k.y, k.z};
      *(LAS v4u*)(s + 3 * SC_STR) = v4u{a0, a1, a2, a3};
      if (t == 255) {
        *(LAS v4u*)(s + 16) = v4u{0u, 0u, 0u, 0u};
        *(LAS v4u*)(s + SC_STR + 16) = v4u{k.w >> 16, 0u, 0u, 0u};
        *(LAS v4u*)(s + 2 * SC_STR + 16) = v4u{k.w, 0u, 0u, 0u};
        *(LAS v4u*)(s + 3 * SC_STR + 16) = v4u{a4, k.w >> 16, 0u, 0u};
      }
    }
    STAMP(6);
    float xf[2][8], un[2][8];
#pragma unroll
    for (int e = 0; e < 8; ++e) {
      const unsigned sa = e < 4 ? st0[e] : st1[e - 4];
      const float ra = __builtin_amdgcn_rcpf(bf_hi(sa)), mr = -bf_lo(sa) * ra;
#pragma unroll
      for (int c = 0; c < 2; ++c) {
        xf[c][e] = (e & 1) ? bf_hi(xv[c][e >> 1]) : bf_lo(xv[c][e >> 1]);
        un[c][e] = __builtin_fmaf(xf[c][e], ra, mr);
      }
    }
#pragma unroll
    for (int c = 0; c < 2; ++c)
      *(LAS v4u*)(sm + K2_U + c * U_STR + 480 + t * 16) =
          v4u{pk_h(un[c][0], un[c][1]), pk_h(un[c][2], un[c][3]), pk_h(un[c][4], un[c][5]), pk_h(un[c][6], un[c][7])};
    lds_arrive(flag);
    SB();
#pragma unroll
    for (int c = 0; c < 2; ++c) {
      const float dk = c ? bf_hi(dsk) : bf_lo(dsk);
#pragma unroll
      for (int e = 0; e < 8; ++e) xf[c][e] = __builtin_fmaf(un[c][e], dk, xf[c][e]);
    }
    const int c = cw >> 1, bq = lane & 15, g = lane >> 4, cp = (bq + 1) & 3, q0 = 4 * (cw & 1);
    unsigned ap0 = K2_S + (c * 4 + cp) * SC_STR + 2 * (2031 - 1792 - 32 * q0 - 96 - bq + 8 * g + cp), ap1 = ap0 + 2048;
    unsigned bp = K2_U + c * U_STR + 2 * (224 - 32 * q0 - 96 + 16 * bq + 8 * g);
    asm volatile("" : "+v"(ap0), "+v"(ap1), "+v"(bp));
    SB();
    lds_wait(flag, 4);
    STAMP(1); CSTAMP(10);
    {
      auto rdA = [&](int qi, int pp) {
        LAS char* pa = sm + (pp < 4 ? ap1 : ap0) + (3 - qi) * 64 + ((7 - pp) & 3) * 512;
        const v2u lo = *(LAS v2u*)pa, hi = *(LAS v2u*)(pa + 8);
        return v4u{lo.x, lo.y, hi.x, hi.y};
      };
      auto rdB = [&](int qi, int s) { return *(LAS v4u*)(sm + bp + (3 - qi) * 64 + s * 512); };
      f32x4 acc[8] = {};
      v4u A[2][8], B[2][8];
      A[0][0] = rdA(0, 0);
#pragma unroll
      for (int s = 0; s < 8; ++s) B[0][s] = rdB(0, s);
#pragma unroll
      for (int pp = 1; pp < 8; ++pp) A[0][pp] = rdA(0, pp);
      SB();
#pragma unroll
      for (int qi = 0; qi < 4; ++qi) {
        const int cu = qi & 1, nx = cu ^ 1;
        int k = 0;
#pragma unroll
        for (int pp = 0; pp < 8; ++pp)
#pragma unroll
          for (int s = 0; s + pp < 8; ++s) {
            acc[pp + s] = mfma16h(A[cu][pp], B[cu][s], acc[pp + s]);
            if (qi < 3 && (k & 1) && k < 32) {
              const int f = k >> 1;
              if (f & 1) B[nx][f >> 1] = rdB(qi + 1, f >> 1); else A[nx][f >> 1] = rdA(qi + 1, f >> 1);
              SB();
            }
            ++k;
          }
      }
      LAS char* yp = sm + K2_Y + cw * 8192 + (16 * bq + 4 * g) * 4;
#pragma unroll
      for (int tau = 0; tau < 8; ++tau) *(LAS f32x4*)(yp + tau * 1024) = acc[tau];
    }
    lds_arrive(flag + 1);
    lds_wait(flag + 1, 4);
    STAMP(2); CSTAMP(11);
#pragma unroll
    for (int cc = 0; cc < 2; ++cc) {
      LAS f32x4* y0 = (LAS f32x4*)(sm + K2_Y + cc * 16384 + t * 32);
      const f32x4 ya = y0[0] + y0[512], yb = y0[1] + y0[513];
      v4u o;
#pragma unroll
      for (int e = 0; e < 4; ++e) {
        const float y0v = e < 2 ? ya[2 * e] : yb[2 * e - 4], y1v = e < 2 ? ya[2 * e + 1] : yb[2 * e - 3];
        o[e] = min(pk_bf(__builtin_fmaf(y0v, isc[cc], xf[cc][2 * e]), __builtin_fmaf(y1v, isc[cc], xf[cc][2 * e + 1])), SENT - 1u);
      }
      st128<16>(rs, (int)OFF_ST + (2 * b + cc) * 4096 + t * 16, o);
    }
    STAMP(3);
    const int so = (int)OFF_ST + (dt * 64 + cw * 16 + (lane >> 3)) * 4096 + lt * 128 + (lane & 7) * 16;
    v4u s0, s1;
    for (int spin = 0; spin < (1 << 20); ++spin) {
      s0 = __builtin_amdgcn_raw_buffer_load_b128(rs, so, 0, 16);
      s1 = __builtin_amdgcn_raw_buffer_load_b128(rs, so + 8 * 4096, 0, 16);
      const unsigned mx = max(max(max(s0.x, s0.y), max(s0.z, s0.w)), max(max(s1.x, s1.y), max(s1.z, s1.w)));
      if (!__any(mx == SENT)) break;
      __builtin_amdgcn_s_sleep(4);
    }
    LAS char* tmp = sm + K2_S + cw * 2048;
    *(LAS v4u*)(tmp + lane * 16) = s0;
    *(LAS v4u*)(tmp + 1024 + lane * 16) = s1;
    {
      const int G = lane >> 4, q = (lane >> 2) & 3, p = lane & 3;
      LAS char* src = tmp + q * 128 + G * 32 + p * 8;
      const v4u v0 = tr_read2(src, src + 512), v1 = tr_read2(src + 1024, src + 1536);
      LAS char* dst = sm + K2_SR + lane * SR_ROW + cw * 32;
      *(LAS v4u*)dst = v0;
      *(LAS v4u*)(dst + 16) = v1;
    }
    STAMP(4);
  }
  WG_BARRIER();
  STAMP(7);
  {
    const int row = tid >> 3, c8 = tid & 7;
    LAS char* rp = sm + K2_RED + row * RED_ROW + c8 * 32;
    f32x4 lo = *(LAS f32x4*)rp, hi = *(LAS f32x4*)(rp + 16);
#pragma unroll
    for (int s = 1; s < 4; ++s) { lo += *(LAS f32x4*)(rp + s * RED_W); hi += *(LAS f32x4*)(rp + s * RED_W + 16); }
    const v4u sv = *(LAS v4u*)(sm + K2_SR + row * SR_ROW + c8 * 16);
    const float ri = __builtin_amdgcn_rcpf(bf_hi(stat)), r2 = ri * ri;
    v4u o;
#pragma unroll
    for (int e = 0; e < 4; ++e) {
      const float b0 = e < 2 ? lo[2 * e] : hi[2 * e - 4], b1 = e < 2 ? lo[2 * e + 1] : hi[2 * e - 3];
      o[e] = pk_bf(__builtin_fmaf(b0, r2, bf_lo(sv[e])), __builtin_fmaf(b1, r2, bf_hi(sv[e])));
    }
    *(v4u*)(a.out + (size_t)(lt * 64 + row) * D_ + dt * 64 + c8 * 8) = o;
  }
  STAMP(8);
  STAMP_DUMP(1);
}

}

extern "C" void kernel_launch(void* const* d_in, const int* in_sizes, int n_in, void* d_out, int out_size, void* d_ws,
                              size_t ws_size, hipStream_t stream) {
  K1Args a1;
  a1.x = (const u16*)d_in[0];
  a1.lAr = (const u16*)d_in[1];
  a1.Ai = (const u16*)d_in[2];
  a1.Cr = (const u16*)d_in[3];
  a1.Ci = (const u16*)d_in[4];
  a1.ldt = (const u16*)d_in[5];
  a1.Wg = (const u16*)d_in[8];
  a1.Wu = (const u16*)d_in[9];
  a1.Wd = (const u16*)d_in[10];
  a1.ws = (char*)d_ws;
  K2Args a2;
  a2.Dsk = (const u16*)d_in[6];
  a2.ws = (char*)d_ws;
  a2.out = (u16*)d_out;
  (void)hipFuncSetAttribute((const void*)k1, hipFuncAttributeMaxDynamicSharedMemorySize, K1_LDS);
  (void)hipFuncSetAttribute((const void*)k2, hipFuncAttributeMaxDynamicSharedMemorySize, K2_LDS);
  k1<<<256, 512, K1_LDS, stream>>>(a1);
  k2<<<256, 512, K2_LDS, stream>>>(a2);
}
```

```cpp
#include <hip/hip_runtime.h>
#include <stdint.h>

namespace {

typedef unsigned short u16;
typedef unsigned v4u __attribute__((ext_vector_type(4)));
typedef unsigned v2u __attribute__((ext_vector_type(2)));
typedef short s16x4 __attribute__((ext_vector_type(4)));
typedef __bf16 bf16x8 __attribute__((ext_vector_type(8)));
typedef __bf16 bf16x2 __attribute__((ext_vector_type(2)));
typedef _Float16 f16x8 __attribute__((ext_vector_type(8)));
typedef _Float16 f16x2 __attribute__((ext_vector_type(2)));
typedef float f32x16 __attribute__((ext_vector_type(16)));
typedef float f32x4 __attribute__((ext_vector_type(4)));
typedef float f32x2 __attribute__((ext_vector_type(2)));
#define LAS __attribute__((address_space(3)))
#define GAS __attribute__((address_space(1)))
#define DEVI __device__ __forceinline__
#define SB() __builtin_amdgcn_sched_barrier(0)
#if defined(__HIP_DEVICE_COMPILE__) && __HIP_DEVICE_COMPILE__
#define NO_PK __attribute__((target("no-packed-fp32-ops")))
#else
#define NO_PK
#endif

constexpr int L_ = 2048, D_ = 512, N_ = 128, F_ = 1376;
constexpr int KS2 = F_ / 16;

constexpr size_t OFF_HT = 0;
constexpr size_t OFF_WDT = (size_t)6 << 20;
constexpr size_t OFF_XT = (size_t)8 << 20;
constexpr size_t OFF_KG = (size_t)10 << 20;
constexpr size_t OFF_ST = (size_t)12 << 20;
constexpr size_t OFF_STATS = (size_t)14 << 20;
constexpr size_t OFF_ISC = OFF_STATS + 16384;
constexpr int WS_BYTES = 15 << 20;
constexpr unsigned SENT = 0xFFFFFFFFu;
#define AUX_EARLY 0
#define AUX_H 0

#define STAMP_DECL
#define STAMP_ARG
#define STAMP_PASS
#define STAMP(idx) do {} while (0)
#define STAMP_DUMP(kern) do {} while (0)
#define CSTAMP(idx) do {} while (0)

DEVI float bf_lo(unsigned u) { return __builtin_bit_cast(float, u << 16); }
DEVI float bf_hi(unsigned u) { return __builtin_bit_cast(float, u & 0xFFFF0000u); }
DEVI unsigned pk_bf(float a, float b) { bf16x2 v = {(__bf16)a, (__bf16)b}; return __builtin_bit_cast(unsigned, v); }
DEVI unsigned pk_h(float a, float b) { f16x2 v = {(_Float16)a, (_Float16)b}; return __builtin_bit_cast(unsigned, v); }
DEVI unsigned umax(unsigned a, unsigned b) { return a > b ? a : b; }
DEVI unsigned umin(unsigned a, unsigned b) { return a < b ? a : b; }
DEVI float relu(float a) { return __builtin_bit_cast(float, max(__builtin_bit_cast(int, a), 0)); }
DEVI float rbf(float a) { return bf_lo(pk_bf(a, a)); }
DEVI float dot2bf(unsigned a, unsigned b, float c) {
  return __builtin_amdgcn_fdot2_f32_bf16(__builtin_bit_cast(bf16x2, a), __builtin_bit_cast(bf16x2, b), c, false);
}
DEVI f32x16 mfma32(v4u a, v4u b, f32x16 c) {
  return __builtin_amdgcn_mfma_f32_32x32x16_bf16(__builtin_bit_cast(bf16x8, a), __builtin_bit_cast(bf16x8, b), c, 0, 0, 0);
}
DEVI f32x4 mfma16b(v4u a, v4u b, f32x4 c) {
  return __builtin_amdgcn_mfma_f32_16x16x32_bf16(__builtin_bit_cast(bf16x8, a), __builtin_bit_cast(bf16x8, b), c, 0, 0, 0);
}
DEVI f32x4 mfma16h(v4u a, v4u b, f32x4 c) {
  return __builtin_amdgcn_mfma_f32_16x16x32_f16(__builtin_bit_cast(f16x8, a), __builtin_bit_cast(f16x8, b), c, 0, 0, 0);
}
DEVI v2u tr_read(LAS char* p) { return __builtin_bit_cast(v2u, __builtin_amdgcn_ds_read_tr16_b64_v4i16((LAS s16x4*)p)); }
DEVI v4u tr_read2(LAS char* p0, LAS char* p1) { v2u a = tr_read(p0), b = tr_read(p1); return v4u{a.x, a.y, b.x, b.y}; }
DEVI void glds16(const void* g, LAS char* l) { __builtin_amdgcn_global_load_lds((const GAS unsigned*)g, (LAS unsigned*)l, 16, 0, 0); }
#define WG_BARRIER() asm volatile("s_waitcnt lgkmcnt(0)\n\ts_barrier" ::: "memory")
template <int N> DEVI void wait_vm() { asm volatile("s_waitcnt vmcnt(%0)" ::"n"(N) : "memory"); }
typedef __amdgpu_buffer_rsrc_t rsrc_t;
DEVI rsrc_t ws_rsrc(char* ws) { return __builtin_amdgcn_make_buffer_rsrc(ws, 0, WS_BYTES, 0x00020000); }
template <int AUX> DEVI void st128(rsrc_t rs, int off, v4u v) { __builtin_amdgcn_raw_buffer_store_b128(v, rs, off, 0, AUX); }

DEVI void lds_arrive(LAS unsigned* cnt) {
  asm volatile("s_waitcnt lgkmcnt(0)" ::: "memory");
  if ((threadIdx.x & 63) == 0) __hip_atomic_fetch_add(cnt, 1u, __ATOMIC_RELAXED, __HIP_MEMORY_SCOPE_WORKGROUP);
}
DEVI void lds_wait(LAS unsigned* cnt, unsigned target) {
  while (__builtin_amdgcn_readfirstlane(__hip_atomic_load(cnt, __ATOMIC_RELAXED, __HIP_MEMORY_SCOPE_WORKGROUP)) < target) {}
  asm volatile("" ::: "memory");
}

DEVI f32x2 cmul(f32x2 a, f32x2 w, f32x2 wr) { return __builtin_elementwise_fma(a.yy, wr, a.xx * w); }
DEVI f32x2 rot90(f32x2 w) { return f32x2{-w.y, w.x}; }

constexpr int SLOT = 40960;
constexpr int K1_SCR = 3 * SLOT;
constexpr int KG_QS = 528, KG_WAVE = 16 * KG_QS + 256;
constexpr int K1_WD = K1_SCR + 4096;
constexpr int K1_CS = K1_SCR + 6144;
constexpr int K1_LDS = K1_SCR + 4 * KG_WAVE;

struct K1Args {
  const u16 *x, *lAr, *Ai, *Cr, *Ci, *ldt, *Wg, *Wu, *Wd;
  char* ws;
};

template <int NU>
DEVI void k1_loader(const K1Args& a, LAS char* sm, int lw, int lane, int it, int u0, int rot STAMP_ARG) {
  constexpr int RS = 64 * NU, NI = 8 + 4 * NU;
  unsigned xo[2], wo[2 * NU];
#pragma unroll
  for (int i = 0; i < 2; ++i) {
    const int row = lw * 64 + i * 8 + (lane >> 3);
    xo[i] = (unsigned)((it * 128 + row) * D_ + (((lane & 7) ^ ((row >> 1) & 7)) << 3)) * 2u;
  }
#pragma unroll
  for (int i = 0; i < 2 * NU; ++i) {
    const int e = i * 64 + lane, row = e / (4 * NU), c = e % (4 * NU);
    wo[i] = (unsigned)((lw * 32 + row) * F_ + u0 * 32 + c * 8) * 2u;
  }
  auto issue = [&](int cc) {
    LAS char* base = sm + (cc % 3) * SLOT;
    const int c = (cc + rot) & 7;
    const char* xs = (const char*)a.x + c * 128;
    const char* g = (const char*)a.Wg + c * (64 * F_ * 2);
    const char* u = (const char*)a.Wu + c * (64 * F_ * 2);
#pragma unroll
    for (int i = 0; i < 8; ++i) glds16(xs + (i >> 1) * (16 * D_ * 2) + xo[i & 1], base + (lw * 8 + i) * 1024);
#pragma unroll
    for (int i = 0; i < 2 * NU; ++i) glds16(g + wo[i], base + 16384 + lw * 32 * RS + i * 1024);
#pragma unroll
    for (int i = 0; i < 2 * NU; ++i) glds16(u + wo[i], base + 16384 + 64 * RS + lw * 32 * RS + i * 1024);
  };
  issue(0); issue(1);
  STAMP(1);
  wait_vm<NI>(); STAMP(2); WG_BARRIER();
  STAMP(3);
  issue(2);
  wait_vm<NI>(); WG_BARRIER();
  {
#pragma unroll
    for (int v = 0; v < 2; ++v) {
      const int bi = min(lw + 2 * v, 2), blk = min((int)blockIdx.x + 256 * bi, 687);
      const char* src = (const char*)a.Wd + ((blk >> 3) * 16 * D_ + (blk & 7) * 64) * 2;
#pragma unroll
      for (int i = 0; i < 2; ++i)
        glds16(src + (unsigned)(((i * 8 + (lane >> 3)) * D_ + (lane & 7) * 8) * 2), sm + K1_WD + bi * KG_WAVE + i * 1024);
    }
  }
  issue(3);
  wait_vm<NI + 4>(); WG_BARRIER();
  issue(4);
  wait_vm<NI>(); WG_BARRIER();
  issue(5);
  wait_vm<NI>(); WG_BARRIER();
  issue(6);
  wait_vm<NI>(); WG_BARRIER();
  issue(7);
  wait_vm<NI>(); WG_BARRIER();
  wait_vm<0>(); STAMP(4); WG_BARRIER();
  STAMP(5);
}

DEVI void k1_misc(const K1Args& a, LAS char* sm, int m, int lane, int it, int ft, int rot STAMP_ARG) {
  const rsrc_t rs = ws_rsrc(a.ws);
  const int b = blockIdx.x, G = lane >> 4, q = (lane >> 2) & 3, p = lane & 3, hh = lane >> 5;
#pragma unroll
  for (int i = 0; i < 4; ++i) st128<AUX_EARLY>(rs, (int)OFF_ST + b * 8192 + (m * 4 + i) * 1024 + lane * 16, v4u{SENT, SENT, SENT, SENT});
  const int cstar = ((ft >> 1) - rot) & 7;
  int slot = 0;
#pragma unroll 1
  for (int cc = 0; cc < 8; ++cc) {
    WG_BARRIER();
    if (cc == cstar) {
      LAS char* base = sm + slot * SLOT;
#pragma unroll
      for (int i = 0; i < 4; ++i) {
        const int cb = i & 1, rb = m * 64 + (i >> 1) * 32 + G * 8, r0 = rb + q, r1 = r0 + 4, sl = (ft & 1) * 4 + cb * 2 + (p >> 1);
        const v4u v = tr_read2(base + r0 * 128 + ((sl ^ ((r0 >> 1) & 7)) << 4) + (p & 1) * 8,
                               base + r1 * 128 + ((sl ^ ((r1 >> 1) & 7)) << 4) + (p & 1) * 8);
        st128<AUX_EARLY>(rs, (int)OFF_XT + (ft * 32 + cb * 16 + (lane & 15)) * 4096 + (it * 128 + rb) * 2, v);
      }
    }
    if (cc == 0) {
      LAS float* t0 = (LAS float*)(sm + K1_SCR + 2 * m * KG_WAVE);
      constexpr int T1 = KG_WAVE / 4;
      const int ko = (int)OFF_KG + (2 * b + m) * 4096 + lane * 16;
#pragma unroll
      for (int c = 0; c < 2; ++c) {
        const int M = 255 - 64 * c - lane;
        const float lo = t0[384 + M] + t0[T1 + 384 + M], hi = t0[385 + M] + t0[T1 + 385 + M];
        const unsigned ll = pk_h(lo, lo);
        st128<AUX_EARLY>(rs, ko + c * 1024, v4u{pk_h(hi, hi), pk_h(hi, (M & 1) ? hi : lo), ll, ll});
      }
      {
        const int i0 = 256 + 2 * (127 - lane);
        const float e0 = t0[i0] + t0[T1 + i0], e1 = t0[i0 + 1] + t0[T1 + i0 + 1], e2 = t0[i0 + 2] + t0[T1 + i0 + 2];
        st128<AUX_EARLY>(rs, ko + 2048, v4u{pk_h(e2, e2), pk_h(e1, e1), pk_h(e1, e0), pk_h(e0, e0)});
      }
      {
        const int M = 63 - lane;
        const bool r1 = M >= 32;
        LAS f32x4* tp = (LAS f32x4*)(t0 + (r1 ? 128 + 4 * M : 8 * M));
        const f32x4 va = tp[0] + tp[T1 / 4], vb = tp[1] + tp[T1 / 4 + 1];
        st128<AUX_EARLY>(rs, ko + 3072,
                         v4u{pk_h(r1 ? vb[0] : vb[3], r1 ? va[3] : vb[2]), pk_h(r1 ? va[2] : vb[1], r1 ? va[2] : vb[0]),
                             pk_h(r1 ? va[2] : va[3], r1 ? va[1] : va[2]), pk_h(r1 ? va[0] : va[1], va[0])});
      }
      if (lane == 0) ((float*)(a.ws + OFF_ISC))[2 * b + m] = t0[768];
    }
    if (cc == 4) {
#pragma unroll
      for (int v = 0; v < 2; ++v) {
        const int bi = m + 2 * v, blk = b + 256 * bi;
        if (blk < 688) {
          LAS char* src = sm + K1_WD + bi * KG_WAVE + (4 * hh + q) * 128 + (G & 1) * 32 + p * 8;
#pragma unroll
          for (int t = 0; t < 2; ++t)
            st128<AUX_EARLY>(rs, (int)OFF_WDT + (((blk & 7) * 2 + t) * KS2 + (blk >> 3)) * 1024 + lane * 16,
                             tr_read2(src + t * 64, src + t * 64 + 8 * 128));
        }
      }
    }
    slot = slot == 2 ? 0 : slot + 1;
  }
  STAMP(5);
}

DEVI void k1_kgen(LAS char* sm, int w, int lane, unsigned lar, unsigned ai, unsigned cr, unsigned ci, unsigned cr2, unsigned ci2, unsigned ld STAMP_ARG) {
  const float LOG2E = 1.4426950408889634f;
  const float dt = rbf(__builtin_amdgcn_exp2f(bf_lo(ld) * LOG2E));
  const float a2 = -rbf(__builtin_amdgcn_exp2f(bf_lo(lar) * LOG2E)) * dt * LOG2E;
  const float bb = bf_lo(ai) * dt;
  const float I2PH = 0.15915494f, I2PL = 6.4206382e-09f;
  const float bh = bb * I2PH, bl = __builtin_fmaf(bb, I2PL, __builtin_fmaf(bb, I2PH, -bh));
  auto zpow = [&](float p) {
    const float mag = __builtin_amdgcn_exp2f(a2 * p);
    const float th = __builtin_fmaf(bl, p, __builtin_amdgcn_fractf(bh * p));
    return f32x2{mag * __builtin_amdgcn_cosf(th), mag * __builtin_amdgcn_sinf(th)};
  };
  unsigned mx = max(max(cr & 0x7FFFu, ci & 0x7FFFu), max(cr2 & 0x7FFFu, ci2 & 0x7FFFu));
  mx = max(mx, (unsigned)__builtin_amdgcn_update_dpp(0, (int)mx, 0x121, 0xF, 0xF, false));
  mx = max(mx, (unsigned)__builtin_amdgcn_update_dpp(0, (int)mx, 0x122, 0xF, 0xF, false));
  mx = max(mx, (unsigned)__builtin_amdgcn_update_dpp(0, (int)mx, 0x124, 0xF, 0xF, false));
  mx = max(mx, (unsigned)__builtin_amdgcn_update_dpp(0, (int)mx, 0x128, 0xF, 0xF, false));
  unsigned E = max(max((unsigned)__builtin_amdgcn_readlane(mx, 0), (unsigned)__builtin_amdgcn_readlane(mx, 16)),
                   max((unsigned)__builtin_amdgcn_readlane(mx, 32), (unsigned)__builtin_amdgcn_readlane(mx, 48))) >> 7;
  E = min(max(E, 8u), 254u);
  const float sc = __builtin_bit_cast(float, (260u - E) << 23);
  const float isc = 2.0f * dt * __builtin_bit_cast(float, (E - 6u) << 23);

  LAS char* b0 = sm + K1_SCR + w * KG_WAVE;
  LAS unsigned* wp = (LAS unsigned*)(b0 + (lane >> 2) * KG_QS + (lane & 3) * 4);
  auto put = [&](int row, f32x2 v) { wp[row * 4] = pk_h(v.x, v.y); };
  LAS char* fp = b0 + (lane >> 4) * KG_QS + (lane & 15) * 16;
  auto frag = [&](int tile, int s) { return *(LAS v4u*)(fp + s * 4 * KG_QS + tile * 256); };

  const f32x2 z8 = zpow(8.f), z8r = rot90(z8);
  f32x2 z1 = zpow(1.f), zw = zpow(128.f);
  z1.y = -z1.y; zw.y = -zw.y;
  const f32x2 z1r = rot90(z1), zwr = rot90(zw);
  f32x2 g = {bf_lo(cr) * sc, -bf_lo(ci) * sc};
  const f32x2 g1 = cmul(g, zw, zwr);
  const f32x2 z16 = cmul(z8, z8, z8r), z16r = rot90(z16);
  STAMP(2);
  {
    f32x2 p = g, q = g1, y0 = {1.f, 0.f}, y1 = z8;
#pragma unroll
    for (int i = 0; i < 8; ++i) {
      put(2 * i, y0); put(2 * i + 1, y1);
      put(16 + i, p); put(24 + i, q);
      if (i < 7) { y0 = i ? cmul(y0, z16, z16r) : z16; y1 = cmul(y1, z16, z16r); p = cmul(p, z1, z1r); q = cmul(q, z1, z1r); }
    }
  }
  v4u yb[4], xa[4];
#pragma unroll
  for (int s = 0; s < 4; ++s) { yb[s] = frag(0, s); xa[s] = frag(1, s); }
  SB();
  const f32x2 z2 = cmul(z1, z1, z1r), z2r = rot90(z2);
  const f32x2 z4 = cmul(z2, z2, z2r), z4r = rot90(z4);
  const f32x2 zw2 = cmul(zw, zw, zwr), zw2r = rot90(zw2);
  f32x2 ge = cmul(g, zw2, zw2r), go = cmul(g1, zw2, zw2r);
  {
    f32x2 p = ge, q = go;
#pragma unroll
    for (int i = 0; i < 4; ++i) { put(i, p); put(4 + i, q); if (i < 3) { p = cmul(p, z2, z2r); q = cmul(q, z2, z2r); } }
  }
#pragma unroll
  for (int aa = 4; aa < 8; aa += 2) {
    ge = cmul(ge, zw2, zw2r); go = cmul(go, zw2, zw2r);
    put(8 + (aa - 4) * 2, ge); put(9 + (aa - 4) * 2, cmul(ge, z4, z4r));
    put(10 + (aa - 4) * 2, go); put(11 + (aa - 4) * 2, cmul(go, z4, z4r));
  }
#pragma unroll
  for (int aa = 8; aa <= 16; aa += 2) {
    ge = cmul(ge, zw2, zw2r); put(16 + aa - 8, ge);
    if (aa < 16) { go = cmul(go, zw2, zw2r); put(17 + aa - 8, go); }
  }
  v4u xb[4], xc[4];
#pragma unroll
  for (int s = 0; s < 4; ++s) { xb[s] = frag(0, s); xc[s] = frag(1, s); }
  SB();
  f32x4 d0 = {}, d1 = {}, d2 = {};
#pragma unroll
  for (int s = 0; s < 4; ++s) d0 = mfma16h(xa[s], yb[s], d0);
#pragma unroll
  for (int s = 0; s < 4; ++s) { d1 = mfma16h(xb[s], yb[s], d1); d2 = mfma16h(xc[s], yb[s], d2); }
  LAS float* tab = (LAS float*)b0;
  const int j = lane & 15, gq = lane >> 4;
  *(LAS f32x4*)(tab + 128 * (gq >> 1) + 4 * (gq & 1) + 8 * j) = d0;
  {
    const int i0 = gq < 2 ? 256 + 64 * gq + 4 * j : 384 + 64 * (gq - 2) + 2 * j, st = gq < 2 ? 2 : 32;
    *(LAS f32x2*)(tab + i0) = f32x2{d1[0], d1[1]};
    *(LAS f32x2*)(tab + i0 + st) = f32x2{d1[2], d1[3]};
  }
#pragma unroll
  for (int i = 0; i < 4; ++i) tab[512 + 64 * gq + 16 * i + j] = d2[i];
  tab[768] = isc;
}

template <int NU> struct Frags { v4u x, w[2 * NU]; };

template <int NU>
DEVI void k1_consumer(const K1Args& a, LAS char* sm, int w, int lane, int it, int ft, int u0 STAMP_ARG) {
  constexpr int RS = 64 * NU, NT = 2 * NU;
  const int ch = 2 * blockIdx.x + (w >> 1), n = ch * N_ + 64 * (w & 1) + lane;
  const unsigned lar = a.lAr[n], ai = a.Ai[n], cr = a.Cr[n], ci = a.Ci[n], cr2 = a.Cr[n ^ 64], ci2 = a.Ci[n ^ 64], ld = a.ldt[ch];
  SB();
  const int r32 = lane & 31, hh = lane >> 5, G = lane >> 4, q = (lane >> 2) & 3, p = lane & 3;
  int xo[4], wo[4];
#pragma unroll
  for (int kk = 0; kk < 4; ++kk) {
    const int ks = (kk + w) & 3;
    xo[kk] = (w * 32 + r32) * 128 + (((2 * ks + hh) ^ ((r32 >> 1) & 7)) << 4);
    wo[kk] = 16384 + (16 * ks + 8 * hh + q) * RS + (G & 1) * 32 + p * 8;
    asm volatile("" : "+v"(xo[kk]), "+v"(wo[kk]));
  }
  f32x16 acc[NT];
  v4u sel[NT + 1];
  Frags<NU> F0, F1;
#pragma unroll
  for (int t = 0; t < NT; ++t) { acc[t] = f32x16{}; asm volatile("" : "+v"(acc[t])); F1.w[t] = v4u{}; asm volatile("" : "+v"(F1.w[t])); }
#pragma unroll
  for (int t = 0; t <= NT; ++t) {
    const unsigned s = (lane & 15) == 2 * t + (G & 1) ? 0x3F803F80u : 0u;
    sel[t] = v4u{s, s, s, s};
    asm volatile("" : "+v"(sel[t]));
  }
  F1.x = v4u{};
  asm volatile("" : "+v"(F1.x));
  f32x4 sums = {};
  float s2a = 0.f, s2b = 0.f;
  SB();
  STAMP(1);
  k1_kgen(sm, w, lane, lar, ai, cr, ci, cr2, ci2, ld STAMP_PASS);
  STAMP(3);

  auto rdw = [&](LAS char* wp, int t) { LAS char* s = wp + (t & 1) * 64 * RS + (t >> 1) * 64; return tr_read2(s, s + 4 * RS); };
  auto step = [&](Frags<NU>& Fn, LAS char* base, int kn, const Frags<NU>& Fc, int kc, bool rd) {
    LAS char* wp = base + wo[kn];
    if (rd) { Fn.x = *(LAS v4u*)(base + xo[kn]); Fn.w[0] = rdw(wp, 0); Fn.w[1] = rdw(wp, 1); }
    SB();
    sums = mfma16b(sel[NT], Fc.x, sums);
    acc[0] = mfma32(Fc.w[0], Fc.x, acc[0]);
    SB();
    if (rd) { Fn.w[2] = rdw(wp, 2); Fn.w[3] = rdw(wp, 3); }
    SB();
    sums = mfma16b(sel[kc], Fc.w[kc], sums);
    acc[1] = mfma32(Fc.w[1], Fc.x, acc[1]);
    SB();
    if (NU == 3) {
      if (rd) { Fn.w[NT - 2] = rdw(wp, NT - 2); Fn.w[NT - 1] = rdw(wp, NT - 1); }
      SB();
      if (kc < 2) sums = mfma16b(sel[NT - 2 + (kc & 1)], Fc.w[NT - 2 + (kc & 1)], sums);
    }
#pragma unroll
    for (int t = 2; t < NT; ++t) acc[t] = mfma32(Fc.w[t], Fc.x, acc[t]);
    if (NU == 2) {
      s2a = dot2bf(Fc.x[0], Fc.x[0], s2a); s2b = dot2bf(Fc.x[1], Fc.x[1], s2b);
      s2a = dot2bf(Fc.x[2], Fc.x[2], s2a); s2b = dot2bf(Fc.x[3], Fc.x[3], s2b);
    }
    SB();
  };
  int slot = 0;
#pragma unroll 1
  for (int cc = 0; cc < 8; ++cc) {
    WG_BARRIER();
    LAS char* base = sm + slot * SLOT;
    step(F0, base, 0, F1, 3, true);
    step(F1, base, 1, F0, 0, true);
    step(F0, base, 2, F1, 1, true);
    step(F1, base, 3, F0, 2, true);
    slot = slot == 2 ? 0 : slot + 1;
  }
  STAMP(4); CSTAMP(11);
  sums = mfma16b(sel[NT], F1.x, sums);
  sums = mfma16b(sel[3], F1.w[3], sums);
  acc[0] = mfma32(F1.w[0], F1.x, acc[0]);
  SB();
  LAS float* csp = (LAS float*)(sm + K1_CS);
#pragma unroll
  for (int i = 0; i < 4; ++i) csp[w * (KG_WAVE / 4) + (4 * G + i) * 16 + (lane & 15)] = sums[i];
  SB();
  acc[1] = mfma32(F1.w[1], F1.x, acc[1]);
  SB();
  WG_BARRIER();
  float cs[NT];
#pragma unroll
  for (int t = 0; t < NT; ++t) cs[t] = csp[2 * hh * (KG_WAVE / 4) + t * 32 + r32] + csp[(2 * hh + 1) * (KG_WAVE / 4) + t * 32 + r32];
  const float s1 = csp[w * (KG_WAVE / 4) + NT * 32 + r32];
#pragma unroll
  for (int t = 2; t < NT; ++t) acc[t] = mfma32(F1.w[t], F1.x, acc[t]);
  const float mean = s1 * (1.0f / D_);
  const unsigned mb = pk_bf(-mean, -mean);
  const v4u mfr = {mb, mb, mb, mb};
#pragma unroll
  for (int t = 0; t < NT; ++t) {
    const float h1 = rbf(cs[t]), r1 = cs[t] - h1, h2 = rbf(r1);
    acc[t] = mfma32(v4u{pk_bf(h1, h2), pk_bf(r1 - h2, 0.f), 0u, 0u}, mfr, acc[t]);
  }
  const rsrc_t rs = ws_rsrc(a.ws);
  const int ho = (int)OFF_HT + ((it * 4 + w) * KS2 + 2 * u0) * 1024 + lane * 16;
#pragma unroll
  for (int u = 0; u < NU; ++u) {
#pragma unroll
    for (int s = 0; s < 2; ++s) {
      v4u o;
#pragma unroll
      for (int e = 0; e < 4; ++e) {
        const int i = 8 * s + 2 * e;
        o[e] = pk_bf(relu(acc[2 * u][i]) * acc[2 * u + 1][i], relu(acc[2 * u][i + 1]) * acc[2 * u + 1][i + 1]);
      }
      st128<AUX_H>(rs, ho + (2 * u + s) * 1024, o);
    }
  }
  if (NU == 2) {
    s2a = dot2bf(F1.x[0], F1.x[0], s2a); s2b = dot2bf(F1.x[1], F1.x[1], s2b);
    s2a = dot2bf(F1.x[2], F1.x[2], s2a); s2b = dot2bf(F1.x[3], F1.x[3], s2b);
    float s2 = s2a + s2b;
    s2 += __shfl_xor(s2, 32);
    const float var = fmaxf(s2 * (1.0f / D_) - mean * mean, 0.f);
    if (ft == 15 && hh == 0)
      ((unsigned*)(a.ws + OFF_STATS))[it * 128 + w * 32 + r32] = pk_bf(mean, __builtin_sqrtf(rbf(rbf(var) + 1e-5f)));
  }
  STAMP(5);
}

#define ROTMODE 0
__global__ __launch_bounds__(512, 2) void k1(K1Args a) {
  extern __shared__ __attribute__((aligned(16))) char smem[];
  LAS char* sm = (LAS char*)smem;
  char* ws = a.ws;
  STAMP_DECL
  STAMP(0);
  const int tid = threadIdx.x, lane = tid & 63, w = __builtin_amdgcn_readfirstlane(tid >> 6);
  const int b = blockIdx.x, xcd = b & 7, r = b >> 3;
  const int it = (xcd & 1) * 8 + (r & 7), ft = (xcd >> 1) * 4 + (r >> 3);
  const int rot = ROTMODE == 0 ? ((r & 7) + 2 * (r >> 3)) & 7 : ROTMODE == 1 ? (2 * ((r & 3) + (r >> 3))) & 7 : ROTMODE == 2 ? (4 * ((r & 1) + (r >> 3))) & 7 : 0;
  if (w >= 6) k1_misc(a, sm, w - 6, lane, it, ft, rot STAMP_PASS);
  else if (ft < 11) {
    if (w >= 4) k1_loader<3>(a, sm, w - 4, lane, it, ft * 3, rot STAMP_PASS);
    else k1_consumer<3>(a, sm, w, lane, it, ft, ft * 3 STAMP_PASS);
  } else {
    if (w >= 4) k1_loader<2>(a, sm, w - 4, lane, it, 11 + ft * 2, rot STAMP_PASS);
    else k1_consumer<2>(a, sm, w, lane, it, ft, 11 + ft * 2 STAMP_PASS);
  }
  STAMP_DUMP(0);
}

constexpr int SC_STR = 4160;
constexpr int K2_S = 0;
constexpr int U_STR = 4608;
constexpr int K2_U = K2_S + 8 * SC_STR;
constexpr int K2_Y = K2_U + 2 * U_STR;
constexpr int RED_ROW = 272, RED_W = 64 * RED_ROW;
constexpr int K2_RED = K2_Y + 4 * 8192;
constexpr int SR_ROW = 144;
constexpr int K2_SR = K2_RED + 4 * RED_W;
constexpr int K2_FLAG = K2_SR + 64 * SR_ROW;
constexpr int K2_LDS = K2_FLAG + 64;
#define RING 10
#define PRIO_C 2
#define PRIO_G 0
#define K2MAP 1

struct K2Args {
  const u16* Dsk;
  char* ws;
  u16* out;
};

__global__ __launch_bounds__(512, 2) NO_PK void k2(K2Args a) {
  extern __shared__ __attribute__((aligned(16))) char smem[];
  LAS char* sm = (LAS char*)smem;
  char* ws = a.ws;
  STAMP_DECL
  STAMP(0);
  const int tid = threadIdx.x, lane = tid & 63, w = __builtin_amdgcn_readfirstlane(tid >> 6);
  const int b = blockIdx.x, xcd = b & 7, r = b >> 3;
  const int lt = K2MAP == 2 ? (xcd & 1) * 16 + (r & 15) : K2MAP == 1 ? (xcd & 3) * 8 + (r & 7) : xcd * 4 + (r & 3);
  const int dt = K2MAP == 2 ? (xcd >> 1) * 2 + (r >> 4) : K2MAP == 1 ? (xcd >> 2) * 4 + (r >> 3) : r >> 2;
  LAS unsigned* flag = (LAS unsigned*)(sm + K2_FLAG);
  unsigned stat;
  const rsrc_t rs = ws_rsrc(ws);
  if (w < 4) {
    if (PRIO_G) __builtin_amdgcn_s_setprio(PRIO_G);
    const int ks0 = w * 22 - (w == 3 ? 1 : 0), n = w < 2 ? 22 : 21;
    const int ho = (int)OFF_HT + (lt * 2 * KS2 + ks0) * 1024, wo = (int)OFF_WDT + (dt * 2 * KS2 + ks0) * 1024;
    const int lo = lane * 16;
    v4u f[RING][4];
    auto ld = [&](int i) __attribute__((always_inline)) {
      f[i % RING][0] = __builtin_amdgcn_raw_buffer_load_b128(rs, lo, ho + i * 1024, 0);
      f[i % RING][1] = __builtin_amdgcn_raw_buffer_load_b128(rs, lo, wo + i * 1024, 0);
      f[i % RING][2] = __builtin_amdgcn_raw_buffer_load_b128(rs, lo, ho + (KS2 + i) * 1024, 0);
      f[i % RING][3] = __builtin_amdgcn_raw_buffer_load_b128(rs, lo, wo + (KS2 + i) * 1024, 0);
      SB();
    };
    if (tid == 0) { flag[0] = 0u; flag[1] = 0u; }
    WG_BARRIER();
    stat = __builtin_amdgcn_raw_buffer_load_b32(rs, (lt * 64 + (tid >> 3)) * 4, (int)OFF_STATS, 0);
#pragma unroll
    for (int i = 0; i < RING; ++i) ld(i);
    STAMP(9);
    f32x16 acc[2][2] = {};
#pragma unroll
    for (int i = 0; i < 22; ++i) {
      if (i < 21 || n == 22) {
        const v4u h0 = f[i % RING][0], w0 = f[i % RING][1], h1 = f[i % RING][2], w1 = f[i % RING][3];
        acc[0][0] = mfma32(w0, h0, acc[0][0]);
        acc[0][1] = mfma32(w0, h1, acc[0][1]);
        acc[1][0] = mfma32(w1, h0, acc[1][0]);
        acc[1][1] = mfma32(w1, h1, acc[1][1]);
      }
      SB();
      if (i == 0) { STAMP(3); CSTAMP(10); } if (i == 5) STAMP(4); if (i == 10) STAMP(5); if (i == 15) STAMP(6);
      if (i + RING < 21 || (i + RING == 21 && n == 22)) ld(i + RING);
      SB();
    }
    STAMP(1); CSTAMP(11);
    LAS char* rp = sm + K2_RED + w * RED_W + (lane & 31) * RED_ROW + (lane >> 5) * 16;
#pragma unroll
    for (int T = 0; T < 2; ++T)
#pragma unroll
      for (int Nn = 0; Nn < 2; ++Nn)
#pragma unroll
        for (int qd = 0; qd < 4; ++qd)
          *(LAS f32x4*)(rp + Nn * 32 * RED_ROW + T * 128 + qd * 32) =
              f32x4{acc[T][Nn][4 * qd], acc[T][Nn][4 * qd + 1], acc[T][Nn][4 * qd + 2], acc[T][Nn][4 * qd + 3]};
    STAMP(2);
  } else {
    if (PRIO_C) __builtin_amdgcn_s_setprio(PRIO_C);
    const int cw = w - 4, t = tid - 256;
    v4u xv[2], kv[2]; v2u kp[2];
    const int t16 = t * 16;
#pragma unroll
    for (int c = 0; c < 2; ++c) {
      kv[c] = __builtin_amdgcn_raw_buffer_load_b128(rs, t16, (int)OFF_KG + (2 * b + c) * 4096, 0);
      kp[c] = __builtin_amdgcn_raw_buffer_load_b64(rs, t16, (int)OFF_KG + (2 * b + c) * 4096 - 8, 0);
    }
    const v4u st0 = __builtin_amdgcn_raw_buffer_load_b128(rs, 2 * t16, (int)OFF_STATS, 0), st1 = __builtin_amdgcn_raw_buffer_load_b128(rs, 2 * t16 + 16, (int)OFF_STATS, 0);
#pragma unroll
    for (int c = 0; c < 2; ++c) xv[c] = __builtin_amdgcn_raw_buffer_load_b128(rs, t16, (int)OFF_XT + (2 * b + c) * 4096, 0);
    SB();
    WG_BARRIER();
    stat = __builtin_amdgcn_raw_buffer_load_b32(rs, (lt * 64 + (tid >> 3)) * 4, (int)OFF_STATS, 0);
    const unsigned dsk = *(const unsigned*)(a.Dsk + 2 * b);
    const f32x2 isc = *(const f32x2*)(ws + OFF_ISC + 8 * b);
    if (t < 60) *(LAS v4u*)(sm + K2_U + (t >= 30 ? U_STR - 480 : 0) + t * 16) = v4u{0u, 0u, 0u, 0u};
    if (t >= 64 && t < 80) *(LAS v4u*)(sm + K2_S + ((t - 64) >> 1) * SC_STR + 4112 + (t & 1) * 16) = v4u{0u, 0u, 0u, 0u};
    STAMP(5);
#pragma unroll
    for (int c = 0; c < 2; ++c) {
      if (t == 0) kp[c] = v2u{0u, 0u};
      const v4u k = kv[c]; const v2u pz = kp[c];
      LAS char* s = sm + K2_S + c * 4 * SC_STR + t * 16;
      const unsigned a0 = __builtin_amdgcn_alignbit(pz.y, pz.x, 16), a1 = __builtin_amdgcn_alignbit(k.x, pz.y, 16),
                     a2 = __builtin_amdgcn_alignbit(k.y, k.x, 16), a3 = __builtin_amdgcn_alignbit(k.z, k.y, 16),
                     a4 = __builtin_amdgcn_alignbit(k.w, k.z, 16);
      *(LAS v4u*)(s) = k;
      *(LAS v4u*)(s + SC_STR) = v4u{a1, a2, a3, a4};
      *(LAS v4u*)(s + 2 * SC_STR) = v4u{pz.y, k.x, k.y, k.z};
      *(LAS v4u*)(s + 3 * SC_STR) = v4u{a0, a1, a2, a3};
      if (t == 255) {
        *(LAS v4u*)(s + 16) = v4u{0u, 0u, 0u, 0u};
        *(LAS v4u*)(s + SC_STR + 16) = v4u{k.w >> 16, 0u, 0u, 0u};
        *(LAS v4u*)(s + 2 * SC_STR + 16) = v4u{k.w, 0u, 0u, 0u};
        *(LAS v4u*)(s + 3 * SC_STR + 16) = v4u{a4, k.w >> 16, 0u, 0u};
      }
    }
    STAMP(6);
    float xf[2][8], un[2][8];
#pragma unroll
    for (int e = 0; e < 8; ++e) {
      const unsigned sa = e < 4 ? st0[e] : st1[e - 4];
      const float ra = __builtin_amdgcn_rcpf(bf_hi(sa)), mr = -bf_lo(sa) * ra;
#pragma unroll
      for (int c = 0; c < 2; ++c) {
        xf[c][e] = (e & 1) ? bf_hi(xv[c][e >> 1]) : bf_lo(xv[c][e >> 1]);
        un[c][e] = __builtin_fmaf(xf[c][e], ra, mr);
      }
    }
#pragma unroll
    for (int c = 0; c < 2; ++c)
      *(LAS v4u*)(sm + K2_U + c * U_STR + 480 + t * 16) =
          v4u{pk_h(un[c][0], un[c][1]), pk_h(un[c][2], un[c][3]), pk_h(un[c][4], un[c][5]), pk_h(un[c][6], un[c][7])};
    lds_arrive(flag);
    SB();
#pragma unroll
    for (int c = 0; c < 2; ++c) {
      const float dk = c ? bf_hi(dsk) : bf_lo(dsk);
#pragma unroll
      for (int e = 0; e < 8; ++e) xf[c][e] = __builtin_fmaf(un[c][e], dk, xf[c][e]);
    }
    const int c = cw >> 1, bq = lane & 15, g = lane >> 4, cp = (bq + 1) & 3, q0 = 4 * (cw & 1);
    unsigned ap0 = K2_S + (c * 4 + cp) * SC_STR + 2 * (2031 - 1792 - 32 * q0 - 96 - bq + 8 * g + cp), ap1 = ap0 + 2048;
    unsigned bp = K2_U + c * U_STR + 2 * (224 - 32 * q0 - 96 + 16 * bq + 8 * g);
    asm volatile("" : "+v"(ap0), "+v"(ap1), "+v"(bp));
    SB();
    lds_wait(flag, 4);
    STAMP(1); CSTAMP(10);
    {
      auto rdA = [&](int qi, int pp) __attribute__((always_inline)) {
        LAS char* pa = sm + (pp < 4 ? ap1 : ap0) + (3 - qi) * 64 + ((7 - pp) & 3) * 512;
        const v2u lo = *(LAS v2u*)pa, hi = *(LAS v2u*)(pa + 8);
        return v4u{lo.x, lo.y, hi.x, hi.y};
      };
      auto rdB = [&](int qi, int s) __attribute__((always_inline)) { return *(LAS v4u*)(sm + bp + (3 - qi) * 64 + s * 512); };
      f32x4 acc[8] = {};
      v4u A[2][8], B[2][8];
      A[0][0] = rdA(0, 0);
#pragma unroll
      for (int s = 0; s < 8; ++s) B[0][s] = rdB(0, s);
#pragma unroll
      for (int pp = 1; pp < 8; ++pp) A[0][pp] = rdA(0, pp);
      SB();
#pragma unroll
      for (int qi = 0; qi < 4; ++qi) {
        const int cu = qi & 1, nx = cu ^ 1;
        int k = 0;
#pragma unroll
        for (int pp = 0; pp < 8; ++pp)
#pragma unroll
          for (int s = 0; s + pp < 8; ++s) {
            acc[pp + s] = mfma16h(A[cu][pp], B[cu][s], acc[pp + s]);
            if (qi < 3 && (k & 1) && k < 32) {
              const int f = k >> 1;
              if (f & 1) B[nx][f >> 1] = rdB(qi + 1, f >> 1); else A[nx][f >> 1] = rdA(qi + 1, f >> 1);
              SB();
            }
            ++k;
          }
      }
      LAS char* yp = sm + K2_Y + cw * 8192 + (16 * bq + 4 * g) * 4;
#pragma unroll
      for (int tau = 0; tau < 8; ++tau) *(LAS f32x4*)(yp + tau * 1024) = acc[tau];
    }
    lds_arrive(flag + 1);
    lds_wait(flag + 1, 4);
    STAMP(2); CSTAMP(11);
#pragma unroll
    for (int cc = 0; cc < 2; ++cc) {
      LAS f32x4* y0 = (LAS f32x4*)(sm + K2_Y + cc * 16384 + t * 32);
      const f32x4 ya = y0[0] + y0[512], yb = y0[1] + y0[513];
      v4u o;
#pragma unroll
      for (int e = 0; e < 4; ++e) {
        const float y0v = e < 2 ? ya[2 * e] : yb[2 * e - 4], y1v = e < 2 ? ya[2 * e + 1] : yb[2 * e - 3];
        o[e] = umin(pk_bf(__builtin_fmaf(y0v, isc[cc], xf[cc][2 * e]), __builtin_fmaf(y1v, isc[cc], xf[cc][2 * e + 1])), SENT - 1u);
      }
      st128<16>(rs, (int)OFF_ST + (2 * b + cc) * 4096 + t * 16, o);
    }
    STAMP(3);
    const int so = (int)OFF_ST + (dt * 64 + cw * 16 + (lane >> 3)) * 4096 + lt * 128 + (lane & 7) * 16;
    v4u s0, s1;
    for (int spin = 0; spin < (1 << 20); ++spin) {
      s0 = __builtin_amdgcn_raw_buffer_load_b128(rs, so, 0, 16);
      s1 = __builtin_amdgcn_raw_buffer_load_b128(rs, so + 8 * 4096, 0, 16);
      const unsigned mx = umax(umax(umax(s0.x, s0.y), umax(s0.z, s0.w)), umax(umax(s1.x, s1.y), umax(s1.z, s1.w)));
      if (!__builtin_amdgcn_ballot_w64(mx == SENT)) break;
      __builtin_amdgcn_s_sleep(4);
    }
    LAS char* tmp = sm + K2_S + cw * 2048;
    *(LAS v4u*)(tmp + lane * 16) = s0;
    *(LAS v4u*)(tmp + 1024 + lane * 16) = s1;
    {
      const int G = lane >> 4, q = (lane >> 2) & 3, p = lane & 3;
      LAS char* src = tmp + q * 128 + G * 32 + p * 8;
      const v4u v0 = tr_read2(src, src + 512), v1 = tr_read2(src + 1024, src + 1536);
      LAS char* dst = sm + K2_SR + lane * SR_ROW + cw * 32;
      *(LAS v4u*)dst = v0;
      *(LAS v4u*)(dst + 16) = v1;
    }
    STAMP(4);
  }
  WG_BARRIER();
  STAMP(7);
  {
    const int row = tid >> 3, c8 = tid & 7;
    LAS char* rp = sm + K2_RED + row * RED_ROW + c8 * 32;
    f32x4 lo = *(LAS f32x4*)rp, hi = *(LAS f32x4*)(rp + 16);
#pragma unroll
    for (int s = 1; s < 4; ++s) { lo += *(LAS f32x4*)(rp + s * RED_W); hi += *(LAS f32x4*)(rp + s * RED_W + 16); }
    const v4u sv = *(LAS v4u*)(sm + K2_SR + row * SR_ROW + c8 * 16);
    const float ri = __builtin_amdgcn_rcpf(bf_hi(stat)), r2 = ri * ri;
    v4u o;
#pragma unroll
    for (int e = 0; e < 4; ++e) {
      const float b0 = e < 2 ? lo[2 * e] : hi[2 * e - 4], b1 = e < 2 ? lo[2 * e + 1] : hi[2 * e - 3];
      o[e] = pk_bf(__builtin_fmaf(b0, r2, bf_lo(sv[e])), __builtin_fmaf(b1, r2, bf_hi(sv[e])));
    }
    *(v4u*)(a.out + (size_t)(lt * 64 + row) * D_ + dt * 64 + c8 * 8) = o;
  }
  STAMP(8);
  STAMP_DUMP(1);
}

}

extern "C" void kernel_launch(void* const* d_in, const int* in_sizes, int n_in, void* d_out, int out_size, void* d_ws,
                              size_t ws_size, hipStream_t stream) {
  K1Args a1;
  a1.x = (const u16*)d_in[0];
  a1.lAr = (const u16*)d_in[1];
  a1.Ai = (const u16*)d_in[2];
  a1.Cr = (const u16*)d_in[3];
  a1.Ci = (const u16*)d_in[4];
  a1.ldt = (const u16*)d_in[5];
  a1.Wg = (const u16*)d_in[8];
  a1.Wu = (const u16*)d_in[9];
  a1.Wd = (const u16*)d_in[10];
  a1.ws = (char*)d_ws;
  K2Args a2;
  a2.Dsk = (const u16*)d_in[6];
  a2.ws = (char*)d_ws;
  a2.out = (u16*)d_out;
  (void)hipFuncSetAttribute((const void*)k1, hipFuncAttributeMaxDynamicSharedMemorySize, K1_LDS);
  (void)hipFuncSetAttribute((const void*)k2, hipFuncAttributeMaxDynamicSharedMemorySize, K2_LDS);
  k1<<<256, 512, K1_LDS, stream>>>(a1);
  k2<<<256, 512, K2_LDS, stream>>>(a2);
}
```

```cpp
#include <hip/hip_runtime.h>
#include <stdint.h>

namespace {

typedef unsigned short u16;
typedef unsigned v4u __attribute__((ext_vector_type(4)));
typedef unsigned v2u __attribute__((ext_vector_type(2)));
typedef short s16x4 __attribute__((ext_vector_type(4)));
typedef __bf16 bf16x8 __attribute__((ext_vector_type(8)));
typedef __bf16 bf16x2 __attribute__((ext_vector_type(2)));
typedef _Float16 f16x8 __attribute__((ext_vector_type(8)));
typedef _Float16 f16x2 __attribute__((ext_vector_type(2)));
typedef float f32x16 __attribute__((ext_vector_type(16)));
typedef float f32x4 __attribute__((ext_vector_type(4)));
typedef float f32x2 __attribute__((ext_vector_type(2)));
#define LAS __attribute__((address_space(3)))
#define DEVI __device__ __forceinline__
#define INL __attribute__((always_inline))
#define SB() __builtin_amdgcn_sched_barrier(0)
#define PIN(x) asm volatile("" : "+v"(x))
#if defined(__HIP_DEVICE_COMPILE__) && __HIP_DEVICE_COMPILE__
#define NO_PK __attribute__((target("no-packed-fp32-ops")))
#else
#define NO_PK
#endif

constexpr int L_ = 2048, D_ = 512, N_ = 128, F_ = 1376;
constexpr int KS2 = F_ / 16;

constexpr int OFF_HT = 0;
constexpr int OFF_WDT = 6 << 20;
constexpr int OFF_XT = 8 << 20;
constexpr int OFF_KG = 10 << 20;
constexpr int OFF_ST = 12 << 20;
constexpr int OFF_STATS = 14 << 20;
constexpr int OFF_ISC = OFF_STATS + 16384;
constexpr int WS_BYTES = 15 << 20;
constexpr unsigned SENT = 0xFFFFFFFFu;
constexpr int AUX_SC1 = 16;
constexpr int AUX_WT = 17;
constexpr unsigned CODEPF = 154u;

DEVI float bf_lo(unsigned u) { return __builtin_bit_cast(float, u << 16); }
DEVI float bf_hi(unsigned u) { return __builtin_bit_cast(float, u & 0xFFFF0000u); }
DEVI unsigned pk_bf(float a, float b) { bf16x2 v = {(__bf16)a, (__bf16)b}; return __builtin_bit_cast(unsigned, v); }
DEVI unsigned pk_h(float a, float b) { f16x2 v = {(_Float16)a, (_Float16)b}; return __builtin_bit_cast(unsigned, v); }
DEVI unsigned umax(unsigned a, unsigned b) { return a > b ? a : b; }
DEVI unsigned umin(unsigned a, unsigned b) { return a < b ? a : b; }
DEVI float relu(float a) { const int i = __builtin_bit_cast(int, a); return __builtin_bit_cast(float, i > 0 ? i : 0); }
DEVI float rbf(float a) { return bf_lo(pk_bf(a, a)); }
DEVI float sadd(float a, float b) { float r; asm("v_add_f32 %0, %1, %2" : "=v"(r) : "v"(a), "v"(b)); return r; }
DEVI float ssub(float a, float b) { float r; asm("v_sub_f32 %0, %1, %2" : "=v"(r) : "v"(a), "v"(b)); return r; }
DEVI float sfma(float a, float b, float c) { float r; asm("v_fma_f32 %0, %1, %2, %3" : "=v"(r) : "v"(a), "v"(b), "v"(c)); return r; }
DEVI f32x16 mfma32(v4u a, v4u b, f32x16 c) {
  return __builtin_amdgcn_mfma_f32_32x32x16_bf16(__builtin_bit_cast(bf16x8, a), __builtin_bit_cast(bf16x8, b), c, 0, 0, 0);
}
DEVI f32x4 mfma16h(v4u a, v4u b, f32x4 c) {
  return __builtin_amdgcn_mfma_f32_16x16x32_f16(__builtin_bit_cast(f16x8, a), __builtin_bit_cast(f16x8, b), c, 0, 0, 0);
}
DEVI v2u tr_read(LAS char* p) { return __builtin_bit_cast(v2u, __builtin_amdgcn_ds_read_tr16_b64_v4i16((LAS s16x4*)p)); }
DEVI v4u tr_read2(LAS char* p0, LAS char* p1) { v2u a = tr_read(p0), b = tr_read(p1); return v4u{a.x, a.y, b.x, b.y}; }
#define GLDS(v, off) "global_load_lds_dwordx4 " v ", %[g] offset:" #off "\n\t"
DEVI void glds_x(const char* g, const unsigned* vo, int l) {
  asm volatile("s_mov_b32 m0, %[l]\n\ts_nop 0\n\t" GLDS("%0", -3632) GLDS("%1", -2592) GLDS("%2", -1552) GLDS("%3", -512)
               GLDS("%4", 528) GLDS("%5", 1568) GLDS("%6", 2608) GLDS("%7", 3648)
               ::"v"(vo[0]), "v"(vo[1]), "v"(vo[2]), "v"(vo[3]), "v"(vo[4]), "v"(vo[5]), "v"(vo[6]), "v"(vo[7]), [g] "s"(g), [l] "s"(l) : "memory");
}
DEVI void glds_x1(const char* g, const unsigned* vo, int l) {
  asm volatile("s_mov_b32 m0, %[l]\n\ts_nop 0\n\t" GLDS("%0", 3648) ::"v"(vo[7]), [g] "s"(g), [l] "s"(l) : "memory");
}
DEVI void glds_w(const char* g, const unsigned* vo, int l) {
  asm volatile("s_mov_b32 m0, %[l]\n\ts_nop 0\n\t" GLDS("%0", -3072) GLDS("%1", -2048) GLDS("%2", -1024) GLDS("%3", 0) GLDS("%4", 1024) GLDS("%5", 2048)
               ::"v"(vo[0]), "v"(vo[1]), "v"(vo[2]), "v"(vo[3]), "v"(vo[4]), "v"(vo[5]), [g] "s"(g), [l] "s"(l) : "memory");
}
#define WG_BARRIER() asm volatile("s_waitcnt lgkmcnt(0)\n\ts_barrier" ::: "memory")
#define WAVE_EXIT() do { asm volatile("s_waitcnt lgkmcnt(0)" ::: "memory"); __builtin_amdgcn_endpgm(); } while (0)
template <int N> DEVI void wait_vm() { asm volatile("s_waitcnt vmcnt(%0)" ::"n"(N) : "memory"); }
typedef __amdgpu_buffer_rsrc_t rsrc_t;
DEVI rsrc_t ws_rsrc(char* ws) { return __builtin_amdgcn_make_buffer_rsrc(ws, 0, WS_BYTES, 0x00020000); }
template <int AUX> DEVI void st128(rsrc_t rs, int off, v4u v) { __builtin_amdgcn_raw_buffer_store_b128(v, rs, off, 0, AUX); }
template <int CTRL> DEVI float dpp_f(float v) { return __builtin_bit_cast(float, __builtin_amdgcn_update_dpp(0, __builtin_bit_cast(int, v), CTRL, 0xF, 0xF, false)); }

DEVI void lds_put(LAS unsigned* p, unsigned v) { asm volatile("ds_write_b32 %0, %1" ::"v"(p), "v"(v) : "memory"); }
DEVI v4u lds_get4(LAS unsigned* p) { v4u r; asm volatile("ds_read_b128 %0, %1\n\ts_waitcnt lgkmcnt(0)" : "=v"(r) : "v"(p) : "memory"); return r; }
DEVI unsigned lds_get1(LAS unsigned* p) { unsigned r; asm volatile("ds_read_b32 %0, %1\n\ts_waitcnt lgkmcnt(0)" : "=v"(r) : "v"(p) : "memory"); return r; }
DEVI int min4(v4u p) { return (int)__builtin_amdgcn_readfirstlane(umin(umin(p.x, p.y), umin(p.z, p.w))); }

DEVI f32x2 cmul(f32x2 a, f32x2 w, f32x2 wr) { return __builtin_elementwise_fma(a.yy, wr, a.xx * w); }
DEVI f32x2 rot90(f32x2 w) { return f32x2{-w.y, w.x}; }

constexpr int XB = 1040;
constexpr int RS = 192;
constexpr int W_G = 16 * XB;
constexpr int W_UP = 64 * RS + 32;
constexpr int S_FULL = W_G + W_UP + 64 * RS;
constexpr int SLOT = S_FULL + 32;
constexpr int K1_SCR = 3 * SLOT;
constexpr int KG_QS = 528, KG_WAVE = 16 * KG_QS + 256;
constexpr int K1_CS = K1_SCR + 6144;
constexpr int K1_FLG = K1_SCR + 4 * KG_WAVE;
constexpr int K1_STP = K1_FLG + 64;
constexpr int K1_LDS = K1_STP + 3 * 512;

struct K1Args {
  const u16 *x, *Wg, *Wu, *lAr, *Ai, *Cr, *Ci, *ldt, *Wd;
  char* ws;
};

DEVI void k1_loader(const K1Args& a, LAS char* sm, int lw, int lane, int it, int ft, int u0, int rot, const unsigned (&xo)[8], const unsigned (&wo)[6]) {
  const char* xb = (const char*)a.x + it * 128 * (D_ * 2);
  const int lb = (int)(size_t)sm + lw * SLOT;
  __builtin_amdgcn_s_setprio(1);
  LAS unsigned* flg = (LAS unsigned*)(sm + K1_FLG);
  LAS char* base = sm + lw * SLOT;
  const rsrc_t rs = ws_rsrc(a.ws);
  const int G = lane >> 4, q = (lane >> 2) & 3, p = lane & 3;
  float piv = 0.f, s1 = 0.f, s2 = 0.f;
  int xto = 0;
  v4u xt[8] = {};
#pragma unroll 1
  for (int cc = lw; cc < 8; cc += 3) {
    const int c = (rot >> (4 * cc)) & 7;
    if (cc >= 3 && ft < 15) while (min4(lds_get4(flg)) < cc - 1) __builtin_amdgcn_s_sleep(2);
    if (cc == 1) __builtin_amdgcn_s_sleep(6);
    if (cc == 2) while (!__builtin_amdgcn_readfirstlane(lds_get1(flg + 8))) __builtin_amdgcn_s_sleep(1);
    if (cc > 0) {
      if (__builtin_expect(ft < 15 || c == 7, 1)) {
        glds_x(xb + c * 128, xo, lb + 3632);
        glds_x(xb + c * 128 + 64 * D_ * 2, xo, lb + 3632 + 8 * XB);
      } else glds_x1(xb + c * 128 + 64 * D_ * 2, xo, lb + 3632 + 8 * XB);
      if (ft < 15) {
        const int wb = c * (64 * F_ * 2) + u0 * 64, wl = lb + W_G + 3072;
        glds_w((const char*)a.Wg + wb, wo, wl);
        glds_w((const char*)a.Wu + wb, wo, wl + W_UP);
        glds_w((const char*)a.Wg + wb + 32 * F_ * 2, wo, wl + 32 * RS);
        glds_w((const char*)a.Wu + wb + 32 * F_ * 2, wo, wl + W_UP + 32 * RS);
      }
      if (cc == 1 || cc == 7) lds_put(flg + (cc == 7 ? 9 : 8), 1u);
    }
    wait_vm<0>();
    if (cc == 0) while (!__builtin_amdgcn_readfirstlane(lds_get1(flg + 7))) __builtin_amdgcn_s_sleep(1);
    lds_put((LAS unsigned*)(base + S_FULL), cc >= 5 ? 100u : (unsigned)(cc + 1));
    {
      const v4u v = *(LAS v4u*)(base + ft * XB + lane * 16);
      if (cc < 3) piv = bf_lo(v[0]);
#pragma unroll
      for (int e = 0; e < 4; ++e) {
        const float d0 = ssub(bf_lo(v[e]), piv), d1 = ssub(bf_hi(v[e]), piv);
        s1 = sadd(sadd(s1, d0), d1);
        s2 = sfma(d1, d1, sfma(d0, d0, s2));
      }
    }
    if (c == (ft >> 1)) {
      LAS char* src = base + G * XB + q * 128 + ((((ft & 1) ^ (q >> 1)) * 4 + (p >> 1)) << 4) + (p & 1) * 8;
#pragma unroll
      for (int i = 0; i < 8; ++i) { LAS char* sp = src + (i >> 1) * 4 * XB + (i & 1) * 32; xt[i] = tr_read2(sp, sp + 512); }
      xto = OFF_XT + (ft * 32 + (lane & 15)) * 4096 + (it * 128 + G * 8) * 2;
    }
    asm volatile("s_waitcnt lgkmcnt(0)" ::: "memory");
  }
  {
    const float rn = lw == 2 ? 1.0f / 16 : 1.0f / 24;
    *(LAS f32x2*)(sm + K1_STP + lw * 512 + lane * 8) = f32x2{sfma(s1, rn, piv), sfma(-s1 * rn, s1, s2)};
    lds_put(flg + 4 + lw, 1u);
  }
  if (__builtin_amdgcn_readfirstlane(xto)) {
    while (!__builtin_amdgcn_readfirstlane(lds_get1(flg + 9))) __builtin_amdgcn_s_sleep(4);
#pragma unroll
    for (int i = 0; i < 8; ++i) st128<AUX_WT>(rs, xto + (i & 1) * 16 * 4096 + (i >> 1) * 64, xt[i]);
  }
}

DEVI void k1_misc(const K1Args& a, LAS char* sm, int lane, int it, int ft) {
  const rsrc_t rs = ws_rsrc(a.ws);
  const int b = blockIdx.x;
  LAS unsigned* flg = (LAS unsigned*)(sm + K1_FLG);
  lds_put(flg + 7, 1u);
  while (min4(lds_get4(flg)) < 1) __builtin_amdgcn_s_sleep(4);
  v4u kx[2][4];
  float isc[2];
#pragma unroll
  for (int m = 0; m < 2; ++m) {
    LAS float* t0 = (LAS float*)(sm + K1_SCR + 2 * m * KG_WAVE);
    constexpr int T1 = KG_WAVE / 4;
#pragma unroll
    for (int c = 0; c < 2; ++c) {
      const int M = 255 - 64 * c - lane;
      const float lo = sadd(t0[384 + M], t0[T1 + 384 + M]), hi = sadd(t0[385 + M], t0[T1 + 385 + M]);
      const unsigned ll = pk_h(lo, lo);
      kx[m][c] = v4u{pk_h(hi, hi), pk_h(hi, (M & 1) ? hi : lo), ll, ll};
    }
    {
      const int i0 = 256 + 2 * (127 - lane);
      const float e0 = sadd(t0[i0], t0[T1 + i0]), e1 = sadd(t0[i0 + 1], t0[T1 + i0 + 1]), e2 = sadd(t0[i0 + 2], t0[T1 + i0 + 2]);
      kx[m][2] = v4u{pk_h(e2, e2), pk_h(e1, e1), pk_h(e1, e0), pk_h(e0, e0)};
    }
    {
      const int M = 63 - lane;
      const bool r1 = M >= 32;
      LAS f32x4* tp = (LAS f32x4*)(t0 + (r1 ? 128 + 4 * M : 8 * M));
      const f32x4 a0 = tp[0], a1 = tp[T1 / 4], b0 = tp[1], b1 = tp[T1 / 4 + 1];
      float va[4], vb[4];
#pragma unroll
      for (int e = 0; e < 4; ++e) { va[e] = sadd(a0[e], a1[e]); vb[e] = sadd(b0[e], b1[e]); }
      kx[m][3] = v4u{pk_h(r1 ? vb[0] : vb[3], r1 ? va[3] : vb[2]), pk_h(r1 ? va[2] : vb[1], r1 ? va[2] : vb[0]),
                     pk_h(r1 ? va[2] : va[3], r1 ? va[1] : va[2]), pk_h(r1 ? va[0] : va[1], va[0])};
    }
    isc[m] = t0[768];
#pragma unroll
    for (int c = 0; c < 4; ++c) PIN(kx[m][c]);
  }
  while (!__builtin_amdgcn_readfirstlane(lds_get1(flg + 9))) __builtin_amdgcn_s_sleep(4);
#pragma unroll
  for (int i = 0; i < 8; ++i) st128<AUX_WT>(rs, OFF_KG + b * 8192 + i * 1024 + lane * 16, kx[i >> 2][i & 3]);
#pragma unroll
  for (int i = 0; i < 8; ++i) st128<AUX_WT>(rs, OFF_ST + b * 8192 + i * 1024 + lane * 16, v4u{SENT, SENT, SENT, SENT});
  __builtin_amdgcn_raw_buffer_store_b64(v2u{__builtin_bit_cast(unsigned, isc[0]), __builtin_bit_cast(unsigned, isc[1])}, rs, lane ? 1 << 30 : OFF_ISC + 8 * b, 0, AUX_WT);
  while (min4(lds_get4(flg + 4)) < 1) __builtin_amdgcn_s_sleep(4);
  {
    LAS f32x2* sp = (LAS f32x2*)(sm + K1_STP) + lane;
    const f32x2 p0 = sp[0], p1 = sp[64], p2 = sp[128];
    auto sum8 = [&](float v) INL {
      v = sadd(v, dpp_f<0xB1>(v)); v = sadd(v, dpp_f<0x4E>(v)); return sadd(v, dpp_f<0x141>(v));
    };
    const float mean = sum8(sfma(p2.x, 16.f, sadd(p0.x, p1.x) * 24.f)) * (1.0f / D_);
    const float d0 = ssub(p0.x, mean), d1 = ssub(p1.x, mean), d2 = ssub(p2.x, mean);
    const float m2 = sfma(d2 * 16.f, d2, sfma(sfma(d1, d1, d0 * d0), 24.f, sadd(sadd(p0.y, p1.y), p2.y)));
    const float var = sum8(m2) * (1.0f / D_);
    __builtin_amdgcn_raw_buffer_store_b32(pk_bf(mean, __builtin_sqrtf(rbf(rbf(__builtin_fmaxf(var, 0.f)) + 1e-5f))), rs,
                                          (lane & 7) ? 1 << 30 : OFF_STATS + ((lane >> 5) * 1024 + (it * 16 + ft) * 4 + ((lane >> 3) & 3)) * 4, 0, AUX_WT);
  }
}

DEVI void k1_kgen(LAS char* sm, int w, int lane, unsigned lar, unsigned ai, unsigned cr, unsigned ci, unsigned cr2, unsigned ci2, unsigned ld) {
  const float LOG2E = 1.4426950408889634f;
  const float dt = rbf(__builtin_amdgcn_exp2f(bf_lo(ld) * LOG2E));
  const float a2 = -rbf(__builtin_amdgcn_exp2f(bf_lo(lar) * LOG2E)) * dt * LOG2E;
  const float bb = bf_lo(ai) * dt;
  const float I2PH = 0.15915494f, I2PL = 6.4206382e-09f;
  const float bh = bb * I2PH, bl = __builtin_fmaf(bb, I2PL, __builtin_fmaf(bb, I2PH, -bh));
  auto zpow = [&](float p) INL {
    const float mag = __builtin_amdgcn_exp2f(a2 * p);
    const float th = __builtin_fmaf(bl, p, __builtin_amdgcn_fractf(bh * p));
    return f32x2{mag * __builtin_amdgcn_cosf(th), mag * __builtin_amdgcn_sinf(th)};
  };
  unsigned mx = umax(umax(cr & 0x7FFFu, ci & 0x7FFFu), umax(cr2 & 0x7FFFu, ci2 & 0x7FFFu));
  mx = umax(mx, (unsigned)__builtin_amdgcn_update_dpp(0, (int)mx, 0x121, 0xF, 0xF, false));
  mx = umax(mx, (unsigned)__builtin_amdgcn_update_dpp(0, (int)mx, 0x122, 0xF, 0xF, false));
  mx = umax(mx, (unsigned)__builtin_amdgcn_update_dpp(0, (int)mx, 0x124, 0xF, 0xF, false));
  mx = umax(mx, (unsigned)__builtin_amdgcn_update_dpp(0, (int)mx, 0x128, 0xF, 0xF, false));
  unsigned E = umax(umax((unsigned)__builtin_amdgcn_readlane(mx, 0), (unsigned)__builtin_amdgcn_readlane(mx, 16)),
                    umax((unsigned)__builtin_amdgcn_readlane(mx, 32), (unsigned)__builtin_amdgcn_readlane(mx, 48))) >> 7;
  E = umin(umax(E, 8u), 254u);
  const float sc = __builtin_bit_cast(float, (260u - E) << 23);
  const float isc = 2.0f * dt * __builtin_bit_cast(float, (E - 6u) << 23);

  LAS char* b0 = sm + K1_SCR + w * KG_WAVE;
  LAS unsigned* wp = (LAS unsigned*)(b0 + (lane >> 2) * KG_QS + (lane & 3) * 4);
  auto put = [&](int row, f32x2 v) INL { wp[row * 4] = pk_h(v.x, v.y); };
  LAS char* fp = b0 + (lane >> 4) * KG_QS + (lane & 15) * 16;
  auto frag = [&](int tile, int s) INL { return *(LAS v4u*)(fp + s * 4 * KG_QS + tile * 256); };

  const f32x2 z8 = zpow(8.f), z8r = rot90(z8);
  f32x2 z1 = zpow(1.f), zw = zpow(128.f);
  z1.y = -z1.y; zw.y = -zw.y;
  const f32x2 z1r = rot90(z1), zwr = rot90(zw);
  const f32x2 g0 = {bf_lo(cr) * sc, -bf_lo(ci) * sc};
  auto cmul4 = [&](f32x2 (&r)[4], f32x2 a0, f32x2 a1, f32x2 a2, f32x2 a3, f32x2 wa, f32x2 war, f32x2 wb, f32x2 wbr) INL {
    const f32x2 t0 = a0.xx * wa, t1 = a1.xx * wa, t2 = a2.xx * wb, t3 = a3.xx * wb;
    SB();
    r[0] = __builtin_elementwise_fma(a0.yy, war, t0); r[1] = __builtin_elementwise_fma(a1.yy, war, t1);
    r[2] = __builtin_elementwise_fma(a2.yy, wbr, t2); r[3] = __builtin_elementwise_fma(a3.yy, wbr, t3);
    SB();
  };
  const f32x2 g1 = cmul(g0, zw, zwr), zw2 = cmul(zw, zw, zwr), z16 = cmul(z8, z8, z8r), z2 = cmul(z1, z1, z1r);
  const f32x2 z16r = rot90(z16), zw2r = rot90(zw2), z2r = rot90(z2);
  auto put4 = [&](LAS unsigned* pa, int ra, int rb, LAS unsigned* pb, int rc, int rd, const f32x2 (&u)[4]) INL {
    unsigned c0 = pk_h(u[0].x, u[0].y), c1 = pk_h(u[1].x, u[1].y), c2 = pk_h(u[2].x, u[2].y), c3 = pk_h(u[3].x, u[3].y);
    asm volatile("" : "+v"(c0), "+v"(c1), "+v"(c2), "+v"(c3));
    pa[ra * 4] = c0; pa[rb * 4] = c1; pb[rc * 4] = c2; pb[rd * 4] = c3;
    SB();
  };
  f32x2 v[4] = {f32x2{1.f, 0.f}, z8, g0, g1};
  {
    LAS unsigned *py = wp, *px = wp;
#pragma unroll
    for (int i = 0; i < 8; ++i) {
      put4(py, 0, 1, px, 16, 24, v);
      cmul4(v, v[0], v[1], v[2], v[3], z16, z16r, z1, z1r);
      py += 8; px += 4;
    }
  }
  cmul4(v, g0, g1, z2, z2, zw2, zw2r, z2, z2r);
  v4u yb[4], xa[4];
#pragma unroll
  for (int s = 0; s < 4; ++s) { yb[s] = frag(0, s); xa[s] = frag(1, s); }
  SB();
  const f32x2 z4 = v[2], z4r = rot90(z4);
  f32x2 ga[4], gb[4];
  put(0, v[0]); put(4, v[1]);
  cmul4(ga, v[0], v[1], v[0], v[1], zw2, zw2r, z2, z2r);
  put4(wp, 8, 10, wp, 1, 5, ga);
  cmul4(gb, ga[0], ga[1], ga[2], ga[3], zw2, zw2r, z2, z2r);
  put4(wp, 12, 14, wp, 2, 6, gb);
  cmul4(v, gb[0], gb[1], gb[2], gb[3], zw2, zw2r, z2, z2r);
  put4(wp, 16, 17, wp, 3, 7, v);
  cmul4(v, v[0], v[1], ga[0], ga[1], zw2, zw2r, z4, z4r);
  put4(wp, 18, 19, wp, 9, 11, v);
  cmul4(v, v[0], v[1], gb[0], gb[1], zw2, zw2r, z4, z4r);
  put4(wp, 20, 21, wp, 13, 15, v);
  v4u xb[4], xc[4];
#pragma unroll
  for (int s = 0; s < 4; ++s) xb[s] = frag(0, s);
  v[0] = cmul(v[0], zw2, zw2r); v[1] = cmul(v[1], zw2, zw2r);
  put(22, v[0]); put(23, v[1]);
  put(24, cmul(v[0], zw2, zw2r));
#pragma unroll
  for (int s = 0; s < 4; ++s) xc[s] = frag(1, s);
  SB();
  f32x4 d0 = {}, d1 = {}, d2 = {};
#pragma unroll
  for (int s = 0; s < 4; ++s) { d0 = mfma16h(xa[s], yb[s], d0); d1 = mfma16h(xb[s], yb[s], d1); }
#pragma unroll
  for (int s = 0; s < 4; ++s) d2 = mfma16h(xc[s], yb[s], d2);
  LAS float* tab = (LAS float*)b0;
  const int j = lane & 15, gq = lane >> 4;
  *(LAS f32x4*)(tab + 128 * (gq >> 1) + 4 * (gq & 1) + 8 * j) = d0;
  {
    const int i0 = gq < 2 ? 256 + 64 * gq + 4 * j : 384 + 64 * (gq - 2) + 2 * j, st = gq < 2 ? 2 : 32;
    *(LAS f32x2*)(tab + i0) = f32x2{d1[0], d1[1]};
    *(LAS f32x2*)(tab + i0 + st) = f32x2{d1[2], d1[3]};
  }
#pragma unroll
  for (int i = 0; i < 4; ++i) tab[512 + 64 * gq + 16 * i + j] = d2[i];
  tab[768] = isc;
}

DEVI void k1_spare(const K1Args& a, LAS char* sm, int cw, int lane, int it) {
  const rsrc_t rs = ws_rsrc(a.ws);
  const rsrc_t rw = __builtin_amdgcn_make_buffer_rsrc((void*)a.Wd, 0, F_ * D_ * 2, 0x00020000);
  const int G = lane >> 4, q = (lane >> 2) & 3, p = lane & 3, hh = lane >> 5;
  const int so = ((lane >> 3) * D_ + (lane & 7) * 8) * 2;
  v4u v[11][2];
#pragma unroll
  for (int i = 0; i < 11; ++i) {
    const int blk = it * 43 + (int)umin(cw + 4 * i, 42), src = ((blk >> 3) * 16 * D_ + (blk & 7) * 64) * 2;
    v[i][0] = __builtin_amdgcn_raw_buffer_load_b128(rw, so, src, 0);
    v[i][1] = __builtin_amdgcn_raw_buffer_load_b128(rw, so, src + 8 * D_ * 2, 0);
    SB();
  }
  LAS char* st = sm + K1_SCR + cw * KG_WAVE + 4096;
  LAS char* src = st + (4 * hh + q) * 128 + (G & 1) * 32 + p * 8;
#pragma unroll
  for (int i = 0; i < 11; ++i) {
    const int blk = it * 43 + (int)umin(cw + 4 * i, 42);
    *(LAS v4u*)(st + lane * 16) = v[i][0];
    *(LAS v4u*)(st + 1024 + lane * 16) = v[i][1];
#pragma unroll
    for (int t = 0; t < 2; ++t)
      st128<AUX_WT>(rs, OFF_WDT + (((blk & 7) * 2 + t) * KS2 + (blk >> 3)) * 1024 + lane * 16, tr_read2(src + t * 64, src + t * 64 + 8 * 128));
  }
}

struct Params { unsigned lar, ai, cr, ci, cr2, ci2, ld; };

DEVI void k1_consumer(const K1Args& a, LAS char* sm, int cw, int lane, int it, int ft, int u0, Params pm) {
  __builtin_amdgcn_s_setprio(3);
  const int mh = cw & 1, nh = cw >> 1;
  const int r32 = lane & 31, hh = lane >> 5, G = lane >> 4, q = (lane >> 2) & 3, p = lane & 3;
  const int lb = (int)(size_t)sm;
  int x0a = lb + (mh * 8 + nh * 4 + (r32 >> 3)) * XB + (r32 & 7) * 128 + ((mh ^ ((r32 >> 1) & 1)) << 6) + hh * 16;
  int x0b = x0a + 64 - (((mh ^ ((r32 >> 1) & 1))) << 7);
  int x1a = x0a + (nh ? -4 * XB : 4 * XB), x1b = x0b + (nh ? -4 * XB : 4 * XB);
  int wa = lb + W_G + (G & 1) * W_UP + (32 * mh + 8 * hh + q) * RS + nh * 96 + p * 8;
  int wb = wa + (mh ? -32 * RS : 32 * RS);
  int fa = lb + S_FULL, pa = lb + K1_FLG + 4 * cw, fl = lb + K1_FLG;
  int ca = lb + K1_CS + cw * 512 + G * 128 + (lane & 15) * 4, cr = lb + K1_CS + (2 * nh + hh) * 512 + r32 * 4;
  int r0 = lb + K1_CS + cw * 512 + 384 + r32 * 4, r1 = lb + K1_CS + (cw ^ 2) * 512 + 384 + r32 * 4;
  PIN(x0a); PIN(x0b); PIN(x1a); PIN(x1b); PIN(wa); PIN(wb); PIN(fa); PIN(pa); PIN(fl); PIN(ca); PIN(cr); PIN(r0); PIN(r1);
  int hof[2][2];
#pragma unroll
  for (int i = 0; i < 2; ++i) {
    const int ho = OFF_HT + ((it * 4 + mh * 2 + (i ^ nh)) * KS2 + 2 * u0 + 3 * nh) * 1024 + lane * 16;
    hof[0][i] = ft == 14 ? 1 << 30 : ho;
    hof[1][i] = ft == 14 && !nh ? 1 << 30 : ho;
    PIN(hof[0][i]); PIN(hof[1][i]);
  }
  f32x16 acc[2][3];
  v4u sel[4];
  int ix = (lane & 1) ? 0xEEEE : 0x4444;
  PIN(ix);
#pragma unroll
  for (int t = 0; t < 3; ++t) { acc[0][t] = f32x16{}; acc[1][t] = f32x16{}; PIN(acc[0][t]); PIN(acc[1][t]); }
#pragma unroll
  for (int t = 0; t < 4; ++t) {
    const unsigned s0 = ((lane & 15) >> 1) == 2 * t ? 0x3F803F80u : 0u, s1 = ((lane & 15) >> 1) == 2 * t + 1 ? 0x3F803F80u : 0u;
    sel[t] = v4u{s0, s0, s1, s1};
    PIN(sel[t]);
  }
  LAS unsigned* flg = (LAS unsigned*)(sm + K1_FLG);
  SB();
  asm volatile("s_waitcnt vmcnt(0)" : "+v"(pm.lar), "+v"(pm.ld), "+v"(pm.ai), "+v"(pm.cr), "+v"(pm.ci), "+v"(pm.cr2), "+v"(pm.ci2) :: "memory");
  k1_kgen(sm, cw, lane, pm.lar, pm.ai, pm.cr, pm.ci, pm.cr2, pm.ci2, pm.ld);
  lds_put(flg + cw, ft == 15 ? 100u : 1u);
  if (__builtin_expect(ft == 15, 0)) { k1_spare(a, sm, cw, lane, it); return; }
  while (__builtin_amdgcn_readfirstlane(lds_get1((LAS unsigned*)(sm + S_FULL))) < 1u) __builtin_amdgcn_s_sleep(1);
  {
    unsigned pv = 1u;
    int sl = 0, nd = 1, d, t;
    asm volatile(
      "ds_read_b128 v[176:179], %[x0a] offset:0\n\t"
      "ds_read_b64_tr_b16 v[208:209], %[wa] offset:0\n\t"
      "ds_read_b64_tr_b16 v[210:211], %[wa] offset:768\n\t"
      "ds_read_b128 v[192:195], %[x1a] offset:0\n\t"
      "ds_read_b64_tr_b16 v[224:225], %[wa] offset:32\n\t"
      "ds_read_b64_tr_b16 v[226:227], %[wa] offset:800\n\t"
      "ds_read_b64_tr_b16 v[240:241], %[wa] offset:64\n\t"
      "ds_read_b64_tr_b16 v[242:243], %[wa] offset:832\n\t"
      "ds_read_b128 v[180:183], %[x0a] offset:32\n\t"
      "ds_read_b64_tr_b16 v[212:213], %[wa] offset:3072\n\t"
      "ds_read_b64_tr_b16 v[214:215], %[wa] offset:3840\n\t"
      "ds_read_b128 v[196:199], %[x1a] offset:32\n\t"
      "ds_read_b64_tr_b16 v[228:229], %[wa] offset:3104\n\t"
      "ds_read_b64_tr_b16 v[230:231], %[wa] offset:3872\n\t"
      "ds_read_b64_tr_b16 v[244:245], %[wa] offset:3136\n\t"
      "ds_read_b64_tr_b16 v[246:247], %[wa] offset:3904\n\t"
      "v_mov_b32 v172, 0\n\t"
      "v_mov_b32 v173, 0\n\t"
      "v_mov_b32 v174, 0\n\t"
      "v_mov_b32 v175, 0\n\t"
      "s_cmp_eq_u32 %[sl], 2\n\t"
      "s_cselect_b32 %[d], %[dn], %[dp]\n\t"
      "s_cselect_b32 %[sl], -1, %[sl]\n\t"
      "s_add_i32 %[sl], %[sl], 1\n\t"
      "v_add_u32 %[x0a], %[d], %[x0a]\n\t"
      "v_add_u32 %[x1a], %[d], %[x1a]\n\t"
      "v_add_u32 %[wa], %[d], %[wa]\n\t"
      "v_add_u32 %[fa], %[d], %[fa]\n\t"
      "s_waitcnt lgkmcnt(8)\n\t"
      "s_branch 5f\n\t"
      "1:\n\t"
      "v_mfma_f32_32x32x16_bf16 %[a00], v[220:223], v[188:191], %[a00]\n\t"
      "ds_read_b128 v[180:183], %[x0a] offset:32\n\t"
      "ds_read_b64_tr_b16 v[212:213], %[wa] offset:3072\n\t"
      "ds_read_b64_tr_b16 v[214:215], %[wa] offset:3840\n\t"
      "v_mfma_f32_32x32x16_bf16 %[a10], v[220:223], v[204:207], %[a10]\n\t"
      "ds_read_b128 v[196:199], %[x1a] offset:32\n\t"
      "s_cmp_eq_u32 %[sl], 2\n\t"
      "s_cselect_b32 %[d], %[dn], %[dp]\n\t"
      "v_mfma_f32_32x32x16_bf16 %[a01], v[236:239], v[188:191], %[a01]\n\t"
      "ds_read_b64_tr_b16 v[228:229], %[wa] offset:3104\n\t"
      "ds_read_b64_tr_b16 v[230:231], %[wa] offset:3872\n\t"
      "s_cselect_b32 %[sl], -1, %[sl]\n\t"
      "v_mfma_f32_32x32x16_bf16 %[a11], v[236:239], v[204:207], %[a11]\n\t"
      "ds_read_b64_tr_b16 v[244:245], %[wa] offset:3136\n\t"
      "ds_read_b64_tr_b16 v[246:247], %[wa] offset:3904\n\t"
      "s_add_i32 %[sl], %[sl], 1\n\t"
      "v_mfma_f32_32x32x16_bf16 %[a02], v[252:255], v[188:191], %[a02]\n\t"
      "v_add_u32 %[x0a], %[d], %[x0a]\n\t"
      "v_add_u32 %[x1a], %[d], %[x1a]\n\t"
      "v_add_u32 %[wa], %[d], %[wa]\n\t"
      "v_add_u32 %[fa], %[d], %[fa]\n\t"
      "v_mfma_f32_32x32x16_bf16 %[a12], v[252:255], v[204:207], %[a12]\n\t"
      "s_waitcnt lgkmcnt(8)\n\t"
      "5:\n\t"
      "v_mfma_f32_32x32x16_bf16 %[a00], v[208:211], v[176:179], %[a00]\n\t"
      "ds_read_b128 v[184:187], %[x0b] offset:0\n\t"
      "ds_read_b64_tr_b16 v[216:217], %[wb] offset:0\n\t"
      "ds_read_b64_tr_b16 v[218:219], %[wb] offset:768\n\t"
      "v_mfma_f32_32x32x16_bf16 %[a10], v[208:211], v[192:195], %[a10]\n\t"
      "ds_read_b128 v[200:203], %[x1b] offset:0\n\t"
      "v_mfma_f32_32x32x16_bf16 %[a01], v[224:227], v[176:179], %[a01]\n\t"
      "ds_read_b64_tr_b16 v[232:233], %[wb] offset:32\n\t"
      "ds_read_b64_tr_b16 v[234:235], %[wb] offset:800\n\t"
      "v_mfma_f32_32x32x16_bf16 %[a11], v[224:227], v[192:195], %[a11]\n\t"
      "ds_read_b64_tr_b16 v[248:249], %[wb] offset:64\n\t"
      "ds_read_b64_tr_b16 v[250:251], %[wb] offset:832\n\t"
      "v_mfma_f32_32x32x16_bf16 %[a02], v[240:243], v[176:179], %[a02]\n\t"
      "ds_read_b32 v171, %[fa]\n\t"
      "v_add_u32 %[pv], 1, %[pv]\n\t"
      "s_add_i32 %[nd], %[nd], 1\n\t"
      "v_mfma_f32_32x32x16_bf16 %[a12], v[240:243], v[192:195], %[a12]\n\t"
      "s_waitcnt lgkmcnt(9)\n\t"
      "v_mfma_f32_32x32x16_bf16 %[a00], v[212:215], v[180:183], %[a00]\n\t"
      "ds_read_b128 v[188:191], %[x0b] offset:32\n\t"
      "ds_read_b64_tr_b16 v[220:221], %[wb] offset:3072\n\t"
      "ds_read_b64_tr_b16 v[222:223], %[wb] offset:3840\n\t"
      "v_mfma_f32_32x32x16_bf16 %[a10], v[212:215], v[196:199], %[a10]\n\t"
      "ds_read_b128 v[204:207], %[x1b] offset:32\n\t"
      "v_smfmac_f32_16x16x64_bf16 v[172:175], %[s3], v[176:183], %[ix]\n\t"
      "v_mfma_f32_32x32x16_bf16 %[a01], v[228:231], v[180:183], %[a01]\n\t"
      "ds_read_b64_tr_b16 v[236:237], %[wb] offset:3104\n\t"
      "ds_read_b64_tr_b16 v[238:239], %[wb] offset:3872\n\t"
      "v_mfma_f32_32x32x16_bf16 %[a11], v[228:231], v[196:199], %[a11]\n\t"
      "ds_read_b64_tr_b16 v[252:253], %[wb] offset:3136\n\t"
      "ds_read_b64_tr_b16 v[254:255], %[wb] offset:3904\n\t"
      "v_smfmac_f32_16x16x64_bf16 v[172:175], %[s0], v[208:215], %[ix]\n\t"
      "v_mfma_f32_32x32x16_bf16 %[a02], v[244:247], v[180:183], %[a02]\n\t"
      "ds_write_b32 %[pa], %[pv]\n\t"
      "s_waitcnt lgkmcnt(9)\n\t"
      "v_readfirstlane_b32 %[t], v171\n\t"
      "v_smfmac_f32_16x16x64_bf16 v[172:175], %[s2], v[240:247], %[ix]\n\t"
      "v_mfma_f32_32x32x16_bf16 %[a12], v[244:247], v[196:199], %[a12]\n\t"
      "s_cmp_lt_u32 %[t], %[nd]\n\t"
      "s_cbranch_scc1 3f\n\t"
      "2:\n\t"
      "v_mfma_f32_32x32x16_bf16 %[a00], v[216:219], v[184:187], %[a00]\n\t"
      "ds_read_b128 v[176:179], %[x0a] offset:0\n\t"
      "ds_read_b64_tr_b16 v[208:209], %[wa] offset:0\n\t"
      "ds_read_b64_tr_b16 v[210:211], %[wa] offset:768\n\t"
      "v_mfma_f32_32x32x16_bf16 %[a10], v[216:219], v[200:203], %[a10]\n\t"
      "ds_read_b128 v[192:195], %[x1a] offset:0\n\t"
      "v_add_u32 %[x0b], %[d], %[x0b]\n\t"
      "v_add_u32 %[x1b], %[d], %[x1b]\n\t"
      "v_mfma_f32_32x32x16_bf16 %[a01], v[232:235], v[184:187], %[a01]\n\t"
      "ds_read_b64_tr_b16 v[224:225], %[wa] offset:32\n\t"
      "ds_read_b64_tr_b16 v[226:227], %[wa] offset:800\n\t"
      "v_add_u32 %[wb], %[d], %[wb]\n\t"
      "v_mfma_f32_32x32x16_bf16 %[a11], v[232:235], v[200:203], %[a11]\n\t"
      "ds_read_b64_tr_b16 v[240:241], %[wa] offset:64\n\t"
      "ds_read_b64_tr_b16 v[242:243], %[wa] offset:832\n\t"
      "s_waitcnt lgkmcnt(8)\n\t"
      "v_smfmac_f32_16x16x64_bf16 v[172:175], %[s3], v[184:191], %[ix]\n\t"
      "v_mfma_f32_32x32x16_bf16 %[a02], v[248:251], v[184:187], %[a02]\n\t"
      "s_cmp_lt_u32 %[nd], 9\n\t"
      "v_smfmac_f32_16x16x64_bf16 v[172:175], %[s1], v[232:239], %[ix]\n\t"
      "v_mfma_f32_32x32x16_bf16 %[a12], v[248:251], v[200:203], %[a12]\n\t"
      "s_cbranch_scc1 1b\n\t"
      "v_mfma_f32_32x32x16_bf16 %[a00], v[220:223], v[188:191], %[a00]\n\t"
      "v_add_f32 v172, v172, v173\n\t"
      "v_add_f32 v174, v174, v175\n\t"
      "v_mov_b32 %[pv], 100\n\t"
      "ds_write2_b32 %[ca], v172, v174 offset1:16\n\t"
      "ds_write_b32 %[pa], %[pv]\n\t"
      "v_mfma_f32_32x32x16_bf16 %[a10], v[220:223], v[204:207], %[a10]\n\t"
      "v_mfma_f32_32x32x16_bf16 %[a01], v[236:239], v[188:191], %[a01]\n\t"
      "ds_read_b128 v[168:171], %[fl]\n\t"
      "v_mfma_f32_32x32x16_bf16 %[a11], v[236:239], v[204:207], %[a11]\n\t"
      "6:\n\t"
      "s_waitcnt lgkmcnt(0)\n\t"
      "v_min_u32 v168, v168, v169\n\t"
      "v_min3_u32 v168, v168, v170, v171\n\t"
      "s_nop 0\n\t"
      "v_readfirstlane_b32 %[t], v168\n\t"
      "s_cmp_ge_u32 %[t], 100\n\t"
      "s_cbranch_scc1 7f\n\t"
      "ds_read_b128 v[168:171], %[fl]\n\t"
      "s_branch 6b\n\t"
      "3:\n\t"
      "ds_read_b32 v171, %[fa]\n\t"
      "s_waitcnt lgkmcnt(0)\n\t"
      "v_readfirstlane_b32 %[t], v171\n\t"
      "s_cmp_lt_u32 %[t], %[nd]\n\t"
      "s_cbranch_scc0 2b\n\t"
      "s_sleep 1\n\t"
      "s_branch 3b\n\t"
      "7:\n\t"
      "ds_read_b32 %[x1b], %[r0]\n\t"
      "ds_read_b32 %[wa], %[r1]\n\t"
      "ds_read_b32 %[x0a], %[cr]\n\t"
      "ds_read_b32 %[x0b], %[cr] offset:128\n\t"
      "ds_read_b32 %[x1a], %[cr] offset:256\n\t"
      "v_mfma_f32_32x32x16_bf16 %[a02], v[252:255], v[188:191], %[a02]\n\t"
      "v_mfma_f32_32x32x16_bf16 %[a12], v[252:255], v[204:207], %[a12]\n\t"
      "s_waitcnt lgkmcnt(0)\n\t"
      : [a00] "+v"(acc[0][0]), [a10] "+v"(acc[1][0]), [a01] "+v"(acc[0][1]), [a11] "+v"(acc[1][1]), [a02] "+v"(acc[0][2]), [a12] "+v"(acc[1][2]), [x0a] "+v"(x0a), [x0b] "+v"(x0b), [x1a] "+v"(x1a), [x1b] "+v"(x1b), [wa] "+v"(wa), [wb] "+v"(wb),
        [fa] "+v"(fa), [pv] "+v"(pv), [sl] "+s"(sl), [nd] "+s"(nd), [d] "=&s"(d), [t] "=&s"(t)
      : [s0] "v"(sel[0]), [s1] "v"(sel[1]), [s2] "v"(sel[2]), [s3] "v"(sel[3]), [ix] "v"(ix), [pa] "v"(pa), [fl] "v"(fl), [ca] "v"(ca), [cr] "v"(cr),
        [r0] "v"(r0), [r1] "v"(r1), [dp] "s"(SLOT), [dn] "n"(-2 * SLOT)
      : "memory", "scc", "v168", "v169", "v170", "v171", "v172", "v173", "v174", "v175", "v176", "v177", "v178", "v179", "v180", "v181", "v182", "v183", "v184", "v185", "v186", "v187", "v188", "v189", "v190", "v191", "v192", "v193", "v194", "v195", "v196", "v197", "v198", "v199", "v200", "v201", "v202", "v203", "v204", "v205", "v206", "v207", "v208", "v209", "v210", "v211", "v212", "v213", "v214", "v215", "v216", "v217", "v218", "v219", "v220", "v221", "v222", "v223", "v224", "v225", "v226", "v227", "v228", "v229", "v230", "v231", "v232", "v233", "v234", "v235", "v236", "v237", "v238", "v239", "v240", "v241", "v242", "v243", "v244", "v245", "v246", "v247", "v248", "v249", "v250", "v251", "v252", "v253", "v254", "v255");
  }
  const float cs[3] = {__builtin_bit_cast(float, x0a), __builtin_bit_cast(float, x0b), __builtin_bit_cast(float, x1a)};
  const float s1[2] = {__builtin_bit_cast(float, x1b), __builtin_bit_cast(float, wa)};
  v4u mfr[2], cf[3];
#pragma unroll
  for (int i = 0; i < 2; ++i) { const float nm = s1[i] * (-1.0f / D_); const unsigned mb = pk_bf(nm, nm); mfr[i] = v4u{mb, mb, mb, mb}; }
#pragma unroll
  for (int t = 0; t < 3; ++t) {
    const unsigned c0 = __builtin_bit_cast(unsigned, cs[t]);
    const float e1 = cs[t] - bf_hi(c0);
    const unsigned c1 = __builtin_bit_cast(unsigned, e1);
    cf[t] = v4u{__builtin_amdgcn_perm(c1, c0, 0x07060302u), __builtin_bit_cast(unsigned, e1 - bf_hi(c1)) >> 16, 0u, 0u};
  }
  SB();
  auto fix = [&](int j) INL { acc[j & 1][j >> 1] = mfma32(cf[j >> 1], mfr[j & 1], acc[j & 1][j >> 1]); PIN(acc[j & 1][j >> 1]); SB(); };
  fix(0); fix(1);
  const rsrc_t rs = ws_rsrc(a.ws);
#pragma unroll
  for (int j = 0; j < 6; ++j) {
    if (j + 2 < 6) fix(j + 2);
    const f32x16& c = acc[j & 1][j >> 1];
    v4u o;
#pragma unroll
    for (int e = 0; e < 4; ++e) o[e] = pk_bf(relu(c[2 * e]) * c[8 + 2 * e], relu(c[2 * e + 1]) * c[9 + 2 * e]);
    st128<AUX_WT>(rs, hof[j >> 1 ? 1 : 0][j & 1] + (j >> 1) * 1024, o);
    SB();
  }
}

__global__ __launch_bounds__(512, 2) void k1(K1Args a) {
  extern __shared__ __attribute__((aligned(16))) char smem[];
  LAS char* sm = (LAS char*)smem;
  const int tid = threadIdx.x, lane = tid & 63, w = __builtin_amdgcn_readfirstlane(tid >> 6);
  const int b = blockIdx.x, xcd = b & 7, r = b >> 3;
  Params pm;
  {
    const unsigned n2 = (unsigned)(b * 256 + (tid & 255)) * 2u;
    asm volatile("s_cmp_lt_u32 %[w], 4\n\ts_cbranch_scc1 1f\n\t"
                 "s_load_dwordx8 s[80:87], %[ka], 0x18\n\ts_load_dwordx2 s[88:89], %[ka], 0x38\n\ts_waitcnt lgkmcnt(0)\n\t"
                 "global_load_ushort %0, %[n], s[80:81]\n\tglobal_load_ushort %1, %[nl], s[88:89]\n\tglobal_load_ushort %2, %[n], s[82:83]\n\t"
                 "global_load_ushort %3, %[n], s[84:85]\n\tglobal_load_ushort %4, %[n], s[86:87]\n\t"
                 "global_load_ushort %5, %[nx], s[84:85]\n\tglobal_load_ushort %6, %[nx], s[86:87]\n\ts_barrier\n\t1:"
                 : "=&v"(pm.lar), "=&v"(pm.ld), "=&v"(pm.ai), "=&v"(pm.cr), "=&v"(pm.ci), "=&v"(pm.cr2), "=&v"(pm.ci2)
                 : [n] "v"(n2), [nx] "v"(n2 ^ 128u), [nl] "v"((n2 >> 8) * 2u), [ka] "s"(__builtin_amdgcn_kernarg_segment_ptr()), [w] "s"(w)
                 : "memory", "scc", "s80", "s81", "s82", "s83", "s84", "s85", "s86", "s87", "s88", "s89");
    static_assert(__builtin_offsetof(K1Args, lAr) == 0x18 && __builtin_offsetof(K1Args, Ci) == 0x30 && __builtin_offsetof(K1Args, ldt) == 0x38, "argument layout");
  }
  unsigned pfv = 0;
  {
    unsigned long long pc;
    asm volatile("s_cmp_lg_u32 %3, 1\n\ts_cbranch_scc1 1f\n\ts_getpc_b64 %1\n\tglobal_load_dword %0, %2, %1\n\t1:"
                 : "+v"(pfv), "=&s"(pc) : "v"(umin((unsigned)(r * 6 + (tid & 7)), CODEPF - 1u) * 128u), "s"(w) : "memory", "scc");
  }
  const int jj = (xcd >> 1) * 30 + r;
  const int it = (xcd & 1) * 8 + (r < 30 ? jj & 7 : (xcd >> 1) * 2 + r - 30), ft = r < 30 ? jj >> 3 : 15, u0 = ft < 14 ? 3 * ft : 40;
  const int c0 = (it + 2 * ft) & 7;
  unsigned xo[8], wo[6];
  const unsigned x0 = (unsigned)((lane >> 3) * D_ * 2 + (((lane & 7) ^ ((lane >> 2) & 4)) << 4) + 3632);
#pragma unroll
  for (int i = 0; i < 8; ++i) { xo[i] = x0 + i * (8 * D_ * 2 - XB); PIN(xo[i]); }
#pragma unroll
  for (int i = 0; i < 3; ++i) {
    const unsigned row = ((i * 64 + lane) * 5462u) >> 16;
    wo[i] = row * (F_ * 2 - 192) + lane * 16 + 3072;
    wo[i + 3] = wo[i] + 16 * (F_ * 2 - 192);
    PIN(wo[i]); PIN(wo[i + 3]);
  }
  const int hf = w & 1, lb = (int)(size_t)sm;
  unsigned xs = (it * 128 + hf * 64) * (D_ * 2) + c0 * 128, wsx = (c0 * 64 + hf * 32) * (F_ * 2) + u0 * 64;
  int lx = lb + 3632 + hf * 8 * XB, lg = lb + W_G + 3072 + hf * 32 * RS, job = (w == 0 || w == 3) ? (ft < 15 ? 2 : 1) : 0;
  int fz = lb + K1_FLG + lane * 4 + (lane >> 4 ? S_FULL - K1_FLG + ((lane >> 4) - 1) * SLOT - lane * 4 : 0);
  asm volatile("" : "+v"(fz), "+s"(xs), "+s"(wsx), "+s"(lx), "+s"(lg), "+s"(job));
  SB();
  asm volatile("s_cmp_ge_u32 %2, 4\n\ts_cbranch_scc1 1f\n\tds_write_b32 %0, %1\n\ts_waitcnt lgkmcnt(0)\n\ts_barrier\n\t1:" ::"v"(fz), "v"(0u), "s"(w) : "memory", "scc");
#define GL2(v, g, off) "global_load_lds_dwordx4 " v ", " g " offset:" #off "\n\t"
#define GLW(g) GL2("%8", g, -3072) GL2("%9", g, -2048) GL2("%10", g, -1024) GL2("%11", g, 0) GL2("%12", g, 1024) GL2("%13", g, 2048)
  asm volatile("s_cmp_eq_u32 %[job], 0\n\ts_cbranch_scc1 1f\n\ts_mov_b32 m0, %[lx]\n\ts_cmp_eq_u32 %[job], 1\n\t"
               GL2("%0", "%[x]", -3632) GL2("%1", "%[x]", -2592) GL2("%2", "%[x]", -1552) GL2("%3", "%[x]", -512)
               GL2("%4", "%[x]", 528) GL2("%5", "%[x]", 1568) GL2("%6", "%[x]", 2608) GL2("%7", "%[x]", 3648)
               "s_cbranch_scc1 1f\n\ts_mov_b32 m0, %[lg]\n\ts_nop 0\n\t" GLW("%[g]") "s_add_u32 m0, %[lg], %[up]\n\ts_nop 0\n\t" GLW("%[u]") "1:"
               ::"v"(xo[0]), "v"(xo[1]), "v"(xo[2]), "v"(xo[3]), "v"(xo[4]), "v"(xo[5]), "v"(xo[6]), "v"(xo[7]),
                 "v"(wo[0]), "v"(wo[1]), "v"(wo[2]), "v"(wo[3]), "v"(wo[4]), "v"(wo[5]),
                 [x] "s"((const char*)a.x + xs), [g] "s"((const char*)a.Wg + wsx), [u] "s"((const char*)a.Wu + wsx), [lx] "s"(lx), [lg] "s"(lg), [job] "s"(job), [up] "n"(W_UP)
               : "memory", "scc");
  asm volatile("" :: "s"(a.Wd), "s"(a.ws));
  const int rot = (int)((0x76543210u + 0x11111111u * (unsigned)c0) & 0x77777777u);
  if (__builtin_expect(w < 3, 1)) {
    k1_loader(a, sm, w, lane, it, ft, u0, rot, xo, wo);
    asm volatile("" :: "v"(pfv));
    WAVE_EXIT();
  }
  if (__builtin_expect(w >= 4, 1)) {
    k1_consumer(a, sm, w - 4, lane, it, ft, u0, pm);
    WAVE_EXIT();
  }
  wait_vm<0>();
  k1_misc(a, sm, lane, it, ft);
}

constexpr int SC_STR = 4256;
constexpr int K2_S = 0;
constexpr int U_STR = 4608;
constexpr int K2_U = K2_S + 8 * SC_STR;
constexpr int K2_Y = K2_U + 2 * U_STR;
constexpr int RED_ROW = 272, RED_W = 64 * RED_ROW;
constexpr int K2_RED = K2_Y + 4 * 8192;
constexpr int SR_ROW = 144;
constexpr int K2_SR = K2_RED + 4 * RED_W;
constexpr int K2_FLAG = K2_SR + 64 * SR_ROW;
constexpr int K2_STAT = K2_FLAG + 64;
constexpr int K2_LDS = K2_STAT + 256;

constexpr int RING = 10, W0 = 5, NE = 6, NT = 5;

struct K2Args {
  char* ws;
  const u16* Dsk;
  u16* out;
};

__global__ __launch_bounds__(512, 2) NO_PK void k2(K2Args a) {
  extern __shared__ __attribute__((aligned(16))) char smem[];
  LAS char* sm = (LAS char*)smem;
  const int tid = threadIdx.x, lane = tid & 63, w = __builtin_amdgcn_readfirstlane(tid >> 6);
  const int b = blockIdx.x, xcd = b & 7, r = b >> 3;
  const int lt = xcd * 4 + (r & 3), dt = r >> 2;
  LAS unsigned* flag = (LAS unsigned*)(sm + K2_FLAG);
  const rsrc_t rs = ws_rsrc(a.ws);
  lds_put(flag + (lane & 15), 0u);
  const bool cv = w < 4;
  const int gw = w - 4, ks0 = gw * 22 - (gw == 3 ? 1 : 0);
  const int ho = OFF_HT + (lt * 2 * KS2 + ks0) * 1024, wo = OFF_WDT + (dt * 2 * KS2 + ks0) * 1024;
  int vo = cv ? tid * 16 : lane * 16;
  int so[8] = {cv ? OFF_KG + b * 8192 : ho, cv ? OFF_KG + b * 8192 - 8 : wo, cv ? OFF_KG + b * 8192 + 4096 : ho + KS2 * 1024,
               cv ? OFF_KG + b * 8192 + 4088 : wo + KS2 * 1024, cv ? OFF_STATS : ho + 1024, cv ? OFF_STATS + 4096 : wo + 1024,
               cv ? OFF_XT + b * 8192 : ho + (KS2 + 1) * 1024, cv ? OFF_XT + b * 8192 + 4096 : wo + (KS2 + 1) * 1024};
  asm volatile("" : "+v"(vo), "+s"(so[0]), "+s"(so[1]), "+s"(so[2]), "+s"(so[3]), "+s"(so[4]), "+s"(so[5]), "+s"(so[6]), "+s"(so[7]));
  WG_BARRIER();
  v4u f[RING][4];
  if (!cv) __builtin_amdgcn_s_sleep(2);
#pragma unroll
  for (int i = 0; i < 8; ++i) f[i >> 2][i & 3] = __builtin_amdgcn_raw_buffer_load_b128(rs, vo, so[i], 0);
  asm volatile("" :: "s"(a.Dsk), "s"(a.out) : "memory");
  if (__builtin_expect(!cv, 1)) {
    const int n = gw < 2 ? 22 : 21;
    int vl = n == 22 ? vo : 1 << 30;
    PIN(vl);
    auto ld1 = [&](int i, int j) INL {
      if (i < 22) f[i % RING][j] = __builtin_amdgcn_raw_buffer_load_b128(rs, (i == 21 ? vl : vo) + (i & 3) * 1024, ((j & 1) ? wo : ho) + ((j >> 1) * KS2 + (i & ~3)) * 1024, 0);
      SB();
    };
    auto ld = [&](int i) INL { ld1(i, 0); ld1(i, 1); ld1(i, 2); ld1(i, 3); };
    f32x16 acc[2][2] = {};
    auto mm = [&](int i, int j) INL { acc[j >> 1][j & 1] = mfma32(f[i % RING][1 + (j & 2)], f[i % RING][(j & 1) * 2], acc[j >> 1][j & 1]); PIN(acc[j >> 1][j & 1]); SB(); };
    __builtin_amdgcn_s_setprio(3);
#pragma unroll
    for (int i = 2; i < W0; ++i) ld(i);
#pragma unroll
    for (int i = 0; i < NE; ++i)
#pragma unroll
      for (int j = 0; j < 4; ++j) { mm(i, j); ld1(W0 + i, j); }
    __builtin_amdgcn_s_setprio(0);
#pragma unroll
    for (int i = W0 + NE; i < NE + RING - 1; ++i) ld(i);
#pragma unroll
    for (int i = NE; i < 22 - NT; ++i) {
#pragma unroll
      for (int j = 0; j < 4; ++j) { mm(i, j); ld1(i + RING - 1, j); }
      if (i + RING - 1 == 21) lds_put(flag + 4 + gw, 1u);
    }
    LAS char* rp = sm + K2_RED + gw * RED_W + (lane & 31) * RED_ROW + (lane >> 5) * 16;
    auto wr = [&](int j, int qd) INL {
      const f32x16& c = acc[j >> 1][j & 1];
      *(LAS f32x4*)(rp + (j & 1) * 32 * RED_ROW + (j >> 1) * 128 + qd * 32) = f32x4{c[4 * qd], c[4 * qd + 1], c[4 * qd + 2], c[4 * qd + 3]};
      SB();
    };
#pragma unroll
    for (int j = 0; j < 4; ++j)
#pragma unroll
      for (int i = 0; i < NT; ++i) {
        mm(22 - NT + i, j);
        if (j > 0 && i >= NT - 4) wr(j - 1, i - (NT - 4));
      }
#pragma unroll
    for (int qd = 0; qd < 4; ++qd) wr(3, qd);
  } else {
    const int cw = w, t = tid, t16 = t * 16;
    __builtin_amdgcn_s_setprio(2);
    const v2u iscb = *(const __attribute__((address_space(4))) v2u*)(size_t)(a.ws + OFF_ISC + 8 * b);
    const unsigned dsk = *(const __attribute__((address_space(4))) unsigned*)(size_t)(a.Dsk + 2 * b);
    if (t < 60) *(LAS v4u*)(sm + K2_U + (t >= 30 ? U_STR - 480 : 0) + t16) = v4u{0u, 0u, 0u, 0u};
    if (t >= 64 && t < 80) *(LAS v4u*)(sm + K2_S + ((t - 64) >> 1) * SC_STR + 4112 + (t & 1) * 16) = v4u{0u, 0u, 0u, 0u};
    const int c = cw >> 1, bq = lane & 15, g = lane >> 4, cp = bq & 3, q0 = 4 * (cw & 1), lb = (int)(size_t)sm;
    int ap0 = lb + K2_S + (c * 4 + cp) * SC_STR + 2 * (2032 - 1792 - 32 * q0 - 96 - bq + 8 * g + cp), ap1 = ap0 + 2048;
    int bp = lb + K2_U + c * U_STR + 2 * (224 - 32 * q0 - 96 + 16 * bq + 8 * g);
    int yp = lb + K2_Y + cw * 8192 + (16 * bq + 4 * g) * 4;
    PIN(ap0); PIN(ap1); PIN(bp); PIN(yp);
#pragma unroll
    for (int cc = 0; cc < 2; ++cc) {
      const v4u k = f[0][2 * cc], pz = f[0][2 * cc + 1];
      LAS char* s = sm + K2_S + cc * 4 * SC_STR + t16;
      const unsigned a0 = __builtin_amdgcn_alignbit(pz.y, pz.x, 16), a1 = __builtin_amdgcn_alignbit(k.x, pz.y, 16),
                     a2 = __builtin_amdgcn_alignbit(k.y, k.x, 16), a3 = __builtin_amdgcn_alignbit(k.z, k.y, 16),
                     a4 = __builtin_amdgcn_alignbit(k.w, k.z, 16);
      *(LAS v4u*)(s + 3 * SC_STR) = pz;
      *(LAS v4u*)(s + SC_STR) = v4u{pz.y, k.x, k.y, k.z};
      *(LAS v4u*)(s) = v4u{a1, a2, a3, a4};
      *(LAS v4u*)(s + 2 * SC_STR) = v4u{a0, a1, a2, a3};
      if (t == 255) {
        *(LAS v4u*)(s + 16) = v4u{k.w >> 16, 0u, 0u, 0u};
        *(LAS v4u*)(s + SC_STR + 16) = v4u{k.w, 0u, 0u, 0u};
        *(LAS v4u*)(s + 2 * SC_STR + 16) = v4u{a4, k.w >> 16, 0u, 0u};
        *(LAS v4u*)(s + 3 * SC_STR + 16) = v4u{k.z, k.w, 0u, 0u};
      }
    }
    float xf[2][8], un[2][8];
#pragma unroll
    for (int e = 0; e < 8; ++e) {
      const unsigned sa = f[1][e >> 2][e & 3];
      const float ra = __builtin_amdgcn_rcpf(bf_hi(sa)), mr = -bf_lo(sa) * ra;
#pragma unroll
      for (int cc = 0; cc < 2; ++cc) {
        const unsigned xw = f[1][2 + cc][e >> 1];
        xf[cc][e] = (e & 1) ? bf_hi(xw) : bf_lo(xw);
        un[cc][e] = __builtin_fmaf(xf[cc][e], ra, mr);
        PIN(un[cc][e]);
      }
    }
#pragma unroll
    for (int cc = 0; cc < 2; ++cc)
      *(LAS v4u*)(sm + K2_U + cc * U_STR + 480 + t16) =
          v4u{pk_h(un[cc][0], un[cc][1]), pk_h(un[cc][2], un[cc][3]), pk_h(un[cc][4], un[cc][5]), pk_h(un[cc][6], un[cc][7])};
    if ((t >> 3) == lt) { *(LAS v4u*)(sm + K2_STAT + (t & 7) * 32) = f[1][0]; *(LAS v4u*)(sm + K2_STAT + (t & 7) * 32 + 16) = f[1][1]; }
    lds_put(flag + cw, 1u);
#pragma unroll
    for (int cc = 0; cc < 2; ++cc) {
      const float dk = cc ? bf_hi(dsk) : bf_lo(dsk);
#pragma unroll
      for (int e = 0; e < 8; ++e) { xf[cc][e] = __builtin_fmaf(un[cc][e], dk, xf[cc][e]); PIN(xf[cc][e]); }
    }
    while (min4(lds_get4(flag)) < 1) {}
    {
      f32x4 acc[8];
      asm volatile(
        "ds_read2_b64 v[128:131], %[a1] offset0:216 offset1:217\n\t"
        "ds_read_b128 v[160:163], %[bp] offset:192\n\t"
        "ds_read_b128 v[164:167], %[bp] offset:704\n\t"
        "ds_read_b128 v[168:171], %[bp] offset:1216\n\t"
        "ds_read_b128 v[172:175], %[bp] offset:1728\n\t"
        "ds_read_b128 v[176:179], %[bp] offset:2240\n\t"
        "ds_read_b128 v[180:183], %[bp] offset:2752\n\t"
        "ds_read_b128 v[184:187], %[bp] offset:3264\n\t"
        "ds_read_b128 v[188:191], %[bp] offset:3776\n\t"
        "ds_read2_b64 v[132:135], %[a1] offset0:152 offset1:153\n\t"
        "ds_read2_b64 v[136:139], %[a1] offset0:88 offset1:89\n\t"
        "ds_read2_b64 v[140:143], %[a1] offset0:24 offset1:25\n\t"
        "ds_read2_b64 v[144:147], %[a0] offset0:216 offset1:217\n\t"
        "ds_read2_b64 v[148:151], %[a0] offset0:152 offset1:153\n\t"
        "ds_read2_b64 v[152:155], %[a0] offset0:88 offset1:89\n\t"
        "ds_read2_b64 v[156:159], %[a0] offset0:24 offset1:25\n\t"
        "s_waitcnt lgkmcnt(14)\n\t"
        "v_mfma_f32_16x16x32_f16 %[c0], v[128:131], v[160:163], 0\n\t"
        "s_waitcnt lgkmcnt(13)\n\t"
        "v_mfma_f32_16x16x32_f16 %[c1], v[128:131], v[164:167], 0\n\t"
        "ds_read2_b64 v[192:195], %[a1] offset0:208 offset1:209\n\t"
        "s_waitcnt lgkmcnt(13)\n\t"
        "v_mfma_f32_16x16x32_f16 %[c2], v[128:131], v[168:171], 0\n\t"
        "s_waitcnt lgkmcnt(12)\n\t"
        "v_mfma_f32_16x16x32_f16 %[c3], v[128:131], v[172:175], 0\n\t"
        "ds_read_b128 v[224:227], %[bp] offset:128\n\t"
        "s_waitcnt lgkmcnt(12)\n\t"
        "v_mfma_f32_16x16x32_f16 %[c4], v[128:131], v[176:179], 0\n\t"
        "s_waitcnt lgkmcnt(11)\n\t"
        "v_mfma_f32_16x16x32_f16 %[c5], v[128:131], v[180:183], 0\n\t"
        "ds_read_b128 v[228:231], %[bp] offset:640\n\t"
        "s_waitcnt lgkmcnt(11)\n\t"
        "v_mfma_f32_16x16x32_f16 %[c6], v[128:131], v[184:187], 0\n\t"
        "s_waitcnt lgkmcnt(10)\n\t"
        "v_mfma_f32_16x16x32_f16 %[c7], v[128:131], v[188:191], 0\n\t"
        "ds_read_b128 v[232:235], %[bp] offset:1152\n\t"
        "s_waitcnt lgkmcnt(10)\n\t"
        "v_mfma_f32_16x16x32_f16 %[c1], v[132:135], v[160:163], %[c1]\n\t"
        "v_mfma_f32_16x16x32_f16 %[c2], v[132:135], v[164:167], %[c2]\n\t"
        "ds_read_b128 v[236:239], %[bp] offset:1664\n\t"
        "v_mfma_f32_16x16x32_f16 %[c3], v[132:135], v[168:171], %[c3]\n\t"
        "v_mfma_f32_16x16x32_f16 %[c4], v[132:135], v[172:175], %[c4]\n\t"
        "ds_read_b128 v[240:243], %[bp] offset:2176\n\t"
        "v_mfma_f32_16x16x32_f16 %[c5], v[132:135], v[176:179], %[c5]\n\t"
        "v_mfma_f32_16x16x32_f16 %[c6], v[132:135], v[180:183], %[c6]\n\t"
        "ds_read_b128 v[244:247], %[bp] offset:2688\n\t"
        "v_mfma_f32_16x16x32_f16 %[c7], v[132:135], v[184:187], %[c7]\n\t"
        "s_waitcnt lgkmcnt(12)\n\t"
        "v_mfma_f32_16x16x32_f16 %[c2], v[136:139], v[160:163], %[c2]\n\t"
        "ds_read_b128 v[248:251], %[bp] offset:3200\n\t"
        "v_mfma_f32_16x16x32_f16 %[c3], v[136:139], v[164:167], %[c3]\n\t"
        "v_mfma_f32_16x16x32_f16 %[c4], v[136:139], v[168:171], %[c4]\n\t"
        "ds_read_b128 v[252:255], %[bp] offset:3712\n\t"
        "v_mfma_f32_16x16x32_f16 %[c5], v[136:139], v[172:175], %[c5]\n\t"
        "v_mfma_f32_16x16x32_f16 %[c6], v[136:139], v[176:179], %[c6]\n\t"
        "ds_read2_b64 v[196:199], %[a1] offset0:144 offset1:145\n\t"
        "v_mfma_f32_16x16x32_f16 %[c7], v[136:139], v[180:183], %[c7]\n\t"
        "s_waitcnt lgkmcnt(14)\n\t"
        "v_mfma_f32_16x16x32_f16 %[c3], v[140:143], v[160:163], %[c3]\n\t"
        "ds_read2_b64 v[200:203], %[a1] offset0:80 offset1:81\n\t"
        "v_mfma_f32_16x16x32_f16 %[c4], v[140:143], v[164:167], %[c4]\n\t"
        "v_mfma_f32_16x16x32_f16 %[c5], v[140:143], v[168:171], %[c5]\n\t"
        "ds_read2_b64 v[204:207], %[a1] offset0:16 offset1:17\n\t"
        "v_mfma_f32_16x16x32_f16 %[c6], v[140:143], v[172:175], %[c6]\n\t"
        "v_mfma_f32_16x16x32_f16 %[c7], v[140:143], v[176:179], %[c7]\n\t"
        "ds_read2_b64 v[208:211], %[a0] offset0:208 offset1:209\n\t"
        "s_waitcnt lgkmcnt(15)\n\t"
        "v_mfma_f32_16x16x32_f16 %[c4], v[144:147], v[160:163], %[c4]\n\t"
        "v_mfma_f32_16x16x32_f16 %[c5], v[144:147], v[164:167], %[c5]\n\t"
        "ds_read2_b64 v[212:215], %[a0] offset0:144 offset1:145\n\t"
        "v_mfma_f32_16x16x32_f16 %[c6], v[144:147], v[168:171], %[c6]\n\t"
        "v_mfma_f32_16x16x32_f16 %[c7], v[144:147], v[172:175], %[c7]\n\t"
        "ds_read2_b64 v[216:219], %[a0] offset0:80 offset1:81\n\t"
        "s_waitcnt lgkmcnt(15)\n\t"
        "v_mfma_f32_16x16x32_f16 %[c5], v[148:151], v[160:163], %[c5]\n\t"
        "v_mfma_f32_16x16x32_f16 %[c6], v[148:151], v[164:167], %[c6]\n\t"
        "ds_read2_b64 v[220:223], %[a0] offset0:16 offset1:17\n\t"
        "v_mfma_f32_16x16x32_f16 %[c7], v[148:151], v[168:171], %[c7]\n\t"
        "s_waitcnt lgkmcnt(15)\n\t"
        "v_mfma_f32_16x16x32_f16 %[c6], v[152:155], v[160:163], %[c6]\n\t"
        "v_mfma_f32_16x16x32_f16 %[c7], v[152:155], v[164:167], %[c7]\n\t"
        "s_waitcnt lgkmcnt(15)\n\t"
        "v_mfma_f32_16x16x32_f16 %[c7], v[156:159], v[160:163], %[c7]\n\t"
        "s_waitcnt lgkmcnt(7)\n\t"
        "v_mfma_f32_16x16x32_f16 %[c0], v[192:195], v[224:227], %[c0]\n\t"
        "v_mfma_f32_16x16x32_f16 %[c1], v[192:195], v[228:231], %[c1]\n\t"
        "ds_read2_b64 v[128:131], %[a1] offset0:200 offset1:201\n\t"
        "v_mfma_f32_16x16x32_f16 %[c2], v[192:195], v[232:235], %[c2]\n\t"
        "v_mfma_f32_16x16x32_f16 %[c3], v[192:195], v[236:239], %[c3]\n\t"
        "ds_read_b128 v[160:163], %[bp] offset:64\n\t"
        "v_mfma_f32_16x16x32_f16 %[c4], v[192:195], v[240:243], %[c4]\n\t"
        "v_mfma_f32_16x16x32_f16 %[c5], v[192:195], v[244:247], %[c5]\n\t"
        "ds_read_b128 v[164:167], %[bp] offset:576\n\t"
        "v_mfma_f32_16x16x32_f16 %[c6], v[192:195], v[248:251], %[c6]\n\t"
        "v_mfma_f32_16x16x32_f16 %[c7], v[192:195], v[252:255], %[c7]\n\t"
        "ds_read_b128 v[168:171], %[bp] offset:1088\n\t"
        "s_waitcnt lgkmcnt(4)\n\t"
        "v_mfma_f32_16x16x32_f16 %[c1], v[196:199], v[224:227], %[c1]\n\t"
        "v_mfma_f32_16x16x32_f16 %[c2], v[196:199], v[228:231], %[c2]\n\t"
        "ds_read_b128 v[172:175], %[bp] offset:1600\n\t"
        "v_mfma_f32_16x16x32_f16 %[c3], v[196:199], v[232:235], %[c3]\n\t"
        "v_mfma_f32_16x16x32_f16 %[c4], v[196:199], v[236:239], %[c4]\n\t"
        "ds_read_b128 v[176:179], %[bp] offset:2112\n\t"
        "v_mfma_f32_16x16x32_f16 %[c5], v[196:199], v[240:243], %[c5]\n\t"
        "v_mfma_f32_16x16x32_f16 %[c6], v[196:199], v[244:247], %[c6]\n\t"
        "ds_read_b128 v[180:183], %[bp] offset:2624\n\t"
        "v_mfma_f32_16x16x32_f16 %[c7], v[196:199], v[248:251], %[c7]\n\t"
        "v_mfma_f32_16x16x32_f16 %[c2], v[200:203], v[224:227], %[c2]\n\t"
        "ds_read_b128 v[184:187], %[bp] offset:3136\n\t"
        "v_mfma_f32_16x16x32_f16 %[c3], v[200:203], v[228:231], %[c3]\n\t"
        "v_mfma_f32_16x16x32_f16 %[c4], v[200:203], v[232:235], %[c4]\n\t"
        "ds_read_b128 v[188:191], %[bp] offset:3648\n\t"
        "v_mfma_f32_16x16x32_f16 %[c5], v[200:203], v[236:239], %[c5]\n\t"
        "v_mfma_f32_16x16x32_f16 %[c6], v[200:203], v[240:243], %[c6]\n\t"
        "ds_read2_b64 v[132:135], %[a1] offset0:136 offset1:137\n\t"
        "v_mfma_f32_16x16x32_f16 %[c7], v[200:203], v[244:247], %[c7]\n\t"
        "v_mfma_f32_16x16x32_f16 %[c3], v[204:207], v[224:227], %[c3]\n\t"
        "ds_read2_b64 v[136:139], %[a1] offset0:72 offset1:73\n\t"
        "v_mfma_f32_16x16x32_f16 %[c4], v[204:207], v[228:231], %[c4]\n\t"
        "v_mfma_f32_16x16x32_f16 %[c5], v[204:207], v[232:235], %[c5]\n\t"
        "ds_read2_b64 v[140:143], %[a1] offset0:8 offset1:9\n\t"
        "v_mfma_f32_16x16x32_f16 %[c6], v[204:207], v[236:239], %[c6]\n\t"
        "v_mfma_f32_16x16x32_f16 %[c7], v[204:207], v[240:243], %[c7]\n\t"
        "ds_read2_b64 v[144:147], %[a0] offset0:200 offset1:201\n\t"
        "v_mfma_f32_16x16x32_f16 %[c4], v[208:211], v[224:227], %[c4]\n\t"
        "v_mfma_f32_16x16x32_f16 %[c5], v[208:211], v[228:231], %[c5]\n\t"
        "ds_read2_b64 v[148:151], %[a0] offset0:136 offset1:137\n\t"
        "v_mfma_f32_16x16x32_f16 %[c6], v[208:211], v[232:235], %[c6]\n\t"
        "v_mfma_f32_16x16x32_f16 %[c7], v[208:211], v[236:239], %[c7]\n\t"
        "ds_read2_b64 v[152:155], %[a0] offset0:72 offset1:73\n\t"
        "v_mfma_f32_16x16x32_f16 %[c5], v[212:215], v[224:227], %[c5]\n\t"
        "v_mfma_f32_16x16x32_f16 %[c6], v[212:215], v[228:231], %[c6]\n\t"
        "ds_read2_b64 v[156:159], %[a0] offset0:8 offset1:9\n\t"
        "v_mfma_f32_16x16x32_f16 %[c7], v[212:215], v[232:235], %[c7]\n\t"
        "v_mfma_f32_16x16x32_f16 %[c6], v[216:219], v[224:227], %[c6]\n\t"
        "v_mfma_f32_16x16x32_f16 %[c7], v[216:219], v[228:231], %[c7]\n\t"
        "v_mfma_f32_16x16x32_f16 %[c7], v[220:223], v[224:227], %[c7]\n\t"
        "s_waitcnt lgkmcnt(7)\n\t"
        "v_mfma_f32_16x16x32_f16 %[c0], v[128:131], v[160:163], %[c0]\n\t"
        "v_mfma_f32_16x16x32_f16 %[c1], v[128:131], v[164:167], %[c1]\n\t"
        "ds_read2_b64 v[192:195], %[a1] offset0:192 offset1:193\n\t"
        "v_mfma_f32_16x16x32_f16 %[c2], v[128:131], v[168:171], %[c2]\n\t"
        "v_mfma_f32_16x16x32_f16 %[c3], v[128:131], v[172:175], %[c3]\n\t"
        "ds_read_b128 v[224:227], %[bp] offset:0\n\t"
        "v_mfma_f32_16x16x32_f16 %[c4], v[128:131], v[176:179], %[c4]\n\t"
        "v_mfma_f32_16x16x32_f16 %[c5], v[128:131], v[180:183], %[c5]\n\t"
        "ds_read2_b64 v[196:199], %[a1] offset0:128 offset1:129\n\t"
        "v_mfma_f32_16x16x32_f16 %[c6], v[128:131], v[184:187], %[c6]\n\t"
        "v_mfma_f32_16x16x32_f16 %[c7], v[128:131], v[188:191], %[c7]\n\t"
        "ds_read_b128 v[228:231], %[bp] offset:512\n\t"
        "s_waitcnt lgkmcnt(4)\n\t"
        "v_mfma_f32_16x16x32_f16 %[c1], v[132:135], v[160:163], %[c1]\n\t"
        "v_mfma_f32_16x16x32_f16 %[c2], v[132:135], v[164:167], %[c2]\n\t"
        "ds_read2_b64 v[200:203], %[a1] offset0:64 offset1:65\n\t"
        "v_mfma_f32_16x16x32_f16 %[c3], v[132:135], v[168:171], %[c3]\n\t"
        "v_mfma_f32_16x16x32_f16 %[c4], v[132:135], v[172:175], %[c4]\n\t"
        "ds_read_b128 v[232:235], %[bp] offset:1024\n\t"
        "v_mfma_f32_16x16x32_f16 %[c5], v[132:135], v[176:179], %[c5]\n\t"
        "v_mfma_f32_16x16x32_f16 %[c6], v[132:135], v[180:183], %[c6]\n\t"
        "ds_read2_b64 v[204:207], %[a1] offset0:0 offset1:1\n\t"
        "v_mfma_f32_16x16x32_f16 %[c7], v[132:135], v[184:187], %[c7]\n\t"
        "v_mfma_f32_16x16x32_f16 %[c2], v[136:139], v[160:163], %[c2]\n\t"
        "ds_read_b128 v[236:239], %[bp] offset:1536\n\t"
        "v_mfma_f32_16x16x32_f16 %[c3], v[136:139], v[164:167], %[c3]\n\t"
        "v_mfma_f32_16x16x32_f16 %[c4], v[136:139], v[168:171], %[c4]\n\t"
        "ds_read2_b64 v[208:211], %[a0] offset0:192 offset1:193\n\t"
        "v_mfma_f32_16x16x32_f16 %[c5], v[136:139], v[172:175], %[c5]\n\t"
        "v_mfma_f32_16x16x32_f16 %[c6], v[136:139], v[176:179], %[c6]\n\t"
        "ds_read_b128 v[240:243], %[bp] offset:2048\n\t"
        "v_mfma_f32_16x16x32_f16 %[c7], v[136:139], v[180:183], %[c7]\n\t"
        "v_mfma_f32_16x16x32_f16 %[c3], v[140:143], v[160:163], %[c3]\n\t"
        "ds_read2_b64 v[212:215], %[a0] offset0:128 offset1:129\n\t"
        "v_mfma_f32_16x16x32_f16 %[c4], v[140:143], v[164:167], %[c4]\n\t"
        "v_mfma_f32_16x16x32_f16 %[c5], v[140:143], v[168:171], %[c5]\n\t"
        "ds_read_b128 v[244:247], %[bp] offset:2560\n\t"
        "v_mfma_f32_16x16x32_f16 %[c6], v[140:143], v[172:175], %[c6]\n\t"
        "v_mfma_f32_16x16x32_f16 %[c7], v[140:143], v[176:179], %[c7]\n\t"
        "ds_read2_b64 v[216:219], %[a0] offset0:64 offset1:65\n\t"
        "v_mfma_f32_16x16x32_f16 %[c4], v[144:147], v[160:163], %[c4]\n\t"
        "v_mfma_f32_16x16x32_f16 %[c5], v[144:147], v[164:167], %[c5]\n\t"
        "ds_read_b128 v[248:251], %[bp] offset:3072\n\t"
        "v_mfma_f32_16x16x32_f16 %[c6], v[144:147], v[168:171], %[c6]\n\t"
        "v_mfma_f32_16x16x32_f16 %[c7], v[144:147], v[172:175], %[c7]\n\t"
        "ds_read2_b64 v[220:223], %[a0] offset0:0 offset1:1\n\t"
        "v_mfma_f32_16x16x32_f16 %[c5], v[148:151], v[160:163], %[c5]\n\t"
        "v_mfma_f32_16x16x32_f16 %[c6], v[148:151], v[164:167], %[c6]\n\t"
        "ds_read_b128 v[252:255], %[bp] offset:3584\n\t"
        "v_mfma_f32_16x16x32_f16 %[c7], v[148:151], v[168:171], %[c7]\n\t"
        "v_mfma_f32_16x16x32_f16 %[c6], v[152:155], v[160:163], %[c6]\n\t"
        "v_mfma_f32_16x16x32_f16 %[c7], v[152:155], v[164:167], %[c7]\n\t"
        "v_mfma_f32_16x16x32_f16 %[c7], v[156:159], v[160:163], %[c7]\n\t"
        "s_waitcnt lgkmcnt(8)\n\t"
        "v_mfma_f32_16x16x32_f16 %[c0], v[192:195], v[224:227], %[c0]\n\t"
        "v_mfma_f32_16x16x32_f16 %[c1], v[192:195], v[228:231], %[c1]\n\t"
        "v_mfma_f32_16x16x32_f16 %[c1], v[196:199], v[224:227], %[c1]\n\t"
        "v_mfma_f32_16x16x32_f16 %[c2], v[192:195], v[232:235], %[c2]\n\t"
        "ds_write_b128 %[yp], %[c0] offset:0\n\t"
        "v_mfma_f32_16x16x32_f16 %[c2], v[196:199], v[228:231], %[c2]\n\t"
        "v_mfma_f32_16x16x32_f16 %[c2], v[200:203], v[224:227], %[c2]\n\t"
        "ds_write_b128 %[yp], %[c1] offset:1024\n\t"
        "s_waitcnt lgkmcnt(2)\n\t"
        "v_mfma_f32_16x16x32_f16 %[c3], v[192:195], v[236:239], %[c3]\n\t"
        "v_mfma_f32_16x16x32_f16 %[c3], v[196:199], v[232:235], %[c3]\n\t"
        "v_mfma_f32_16x16x32_f16 %[c3], v[200:203], v[228:231], %[c3]\n\t"
        "ds_write_b128 %[yp], %[c2] offset:2048\n\t"
        "v_mfma_f32_16x16x32_f16 %[c3], v[204:207], v[224:227], %[c3]\n\t"
        "v_mfma_f32_16x16x32_f16 %[c4], v[192:195], v[240:243], %[c4]\n\t"
        "v_mfma_f32_16x16x32_f16 %[c4], v[196:199], v[236:239], %[c4]\n\t"
        "v_mfma_f32_16x16x32_f16 %[c4], v[200:203], v[232:235], %[c4]\n\t"
        "ds_write_b128 %[yp], %[c3] offset:3072\n\t"
        "v_mfma_f32_16x16x32_f16 %[c4], v[204:207], v[228:231], %[c4]\n\t"
        "v_mfma_f32_16x16x32_f16 %[c4], v[208:211], v[224:227], %[c4]\n\t"
        "v_mfma_f32_16x16x32_f16 %[c5], v[192:195], v[244:247], %[c5]\n\t"
        "v_mfma_f32_16x16x32_f16 %[c5], v[196:199], v[240:243], %[c5]\n\t"
        "v_mfma_f32_16x16x32_f16 %[c5], v[200:203], v[236:239], %[c5]\n\t"
        "ds_write_b128 %[yp], %[c4] offset:4096\n\t"
        "v_mfma_f32_16x16x32_f16 %[c5], v[204:207], v[232:235], %[c5]\n\t"
        "v_mfma_f32_16x16x32_f16 %[c5], v[208:211], v[228:231], %[c5]\n\t"
        "v_mfma_f32_16x16x32_f16 %[c5], v[212:215], v[224:227], %[c5]\n\t"
        "v_mfma_f32_16x16x32_f16 %[c6], v[192:195], v[248:251], %[c6]\n\t"
        "v_mfma_f32_16x16x32_f16 %[c6], v[196:199], v[244:247], %[c6]\n\t"
        "v_mfma_f32_16x16x32_f16 %[c6], v[200:203], v[240:243], %[c6]\n\t"
        "ds_write_b128 %[yp], %[c5] offset:5120\n\t"
        "v_mfma_f32_16x16x32_f16 %[c6], v[204:207], v[236:239], %[c6]\n\t"
        "v_mfma_f32_16x16x32_f16 %[c6], v[208:211], v[232:235], %[c6]\n\t"
        "v_mfma_f32_16x16x32_f16 %[c6], v[212:215], v[228:231], %[c6]\n\t"
        "v_mfma_f32_16x16x32_f16 %[c6], v[216:219], v[224:227], %[c6]\n\t"
        "v_mfma_f32_16x16x32_f16 %[c7], v[192:195], v[252:255], %[c7]\n\t"
        "v_mfma_f32_16x16x32_f16 %[c7], v[196:199], v[248:251], %[c7]\n\t"
        "v_mfma_f32_16x16x32_f16 %[c7], v[200:203], v[244:247], %[c7]\n\t"
        "ds_write_b128 %[yp], %[c6] offset:6144\n\t"
        "v_mfma_f32_16x16x32_f16 %[c7], v[204:207], v[240:243], %[c7]\n\t"
        "v_mfma_f32_16x16x32_f16 %[c7], v[208:211], v[236:239], %[c7]\n\t"
        "v_mfma_f32_16x16x32_f16 %[c7], v[212:215], v[232:235], %[c7]\n\t"
        "v_mfma_f32_16x16x32_f16 %[c7], v[216:219], v[228:231], %[c7]\n\t"
        "v_mfma_f32_16x16x32_f16 %[c7], v[220:223], v[224:227], %[c7]\n\t"
        "s_nop 7\n\t"
        "s_nop 1\n\t"
        "ds_write_b128 %[yp], %[c7] offset:7168\n\t"
        : [c0] "=&v"(acc[0]), [c1] "=&v"(acc[1]), [c2] "=&v"(acc[2]), [c3] "=&v"(acc[3]), [c4] "=&v"(acc[4]), [c5] "=&v"(acc[5]), [c6] "=&v"(acc[6]), [c7] "=&v"(acc[7])
        : [a0] "v"(ap0), [a1] "v"(ap1), [bp] "v"(bp), [yp] "v"(yp)
        : "memory", "v128", "v129", "v130", "v131", "v132", "v133", "v134", "v135", "v136", "v137", "v138", "v139", "v140", "v141", "v142", "v143", "v144", "v145", "v146", "v147", "v148", "v149", "v150", "v151", "v152", "v153", "v154", "v155", "v156", "v157", "v158", "v159", "v160", "v161", "v162", "v163", "v164", "v165", "v166", "v167", "v168", "v169", "v170", "v171", "v172", "v173", "v174", "v175", "v176", "v177", "v178", "v179", "v180", "v181", "v182", "v183", "v184", "v185", "v186", "v187", "v188", "v189", "v190", "v191", "v192", "v193", "v194", "v195", "v196", "v197", "v198", "v199", "v200", "v201", "v202", "v203", "v204", "v205", "v206", "v207", "v208", "v209", "v210", "v211", "v212", "v213", "v214", "v215", "v216", "v217", "v218", "v219", "v220", "v221", "v222", "v223", "v224", "v225", "v226", "v227", "v228", "v229", "v230", "v231", "v232", "v233", "v234", "v235", "v236", "v237", "v238", "v239", "v240", "v241", "v242", "v243", "v244", "v245", "v246", "v247", "v248", "v249", "v250", "v251", "v252", "v253", "v254", "v255");
    }
    lds_put(flag + cw, 2u);
    while (min4(lds_get4(flag)) < 2) {}
#pragma unroll
    for (int cc = 0; cc < 2; ++cc) {
      LAS f32x4* y0 = (LAS f32x4*)(sm + K2_Y + cc * 16384 + t * 32);
      const f32x4 ya = y0[0] + y0[512], yb = y0[1] + y0[513];
      const unsigned iscu = cc ? iscb.y : iscb.x;
      const float isc = __builtin_bit_cast(float, iscu);
      v4u o;
#pragma unroll
      for (int e = 0; e < 4; ++e) {
        const float y0v = e < 2 ? ya[2 * e] : yb[2 * e - 4], y1v = e < 2 ? ya[2 * e + 1] : yb[2 * e - 3];
        o[e] = umin(pk_bf(__builtin_fmaf(y0v, isc, xf[cc][2 * e]), __builtin_fmaf(y1v, isc, xf[cc][2 * e + 1])), SENT - 1u);
      }
      st128<AUX_SC1>(rs, OFF_ST + (2 * b + cc) * 4096 + t16, o);
    }
    const int sto = OFF_ST + (dt * 64 + cw * 16 + (lane >> 3)) * 4096 + lt * 128 + (lane & 7) * 16;
    v4u s0, s1;
    while (min4(lds_get4(flag + 4)) < 1) __builtin_amdgcn_s_sleep(4);
#pragma unroll 1
    for (int spin = 0; spin < (1 << 16); ++spin) {
      s0 = __builtin_amdgcn_raw_buffer_load_b128(rs, sto, 0, AUX_SC1);
      s1 = __builtin_amdgcn_raw_buffer_load_b128(rs, sto + 8 * 4096, 0, AUX_SC1);
      const unsigned mx = umax(umax(umax(s0.x, s0.y), umax(s0.z, s0.w)), umax(umax(s1.x, s1.y), umax(s1.z, s1.w)));
      if (!__builtin_amdgcn_ballot_w64(mx == SENT)) break;
      __builtin_amdgcn_s_sleep(4);
    }
    LAS char* tmp = sm + K2_S + cw * 2048;
    *(LAS v4u*)(tmp + lane * 16) = s0;
    *(LAS v4u*)(tmp + 1024 + lane * 16) = s1;
    {
      const int G = lane >> 4, q = (lane >> 2) & 3, p = lane & 3;
      LAS char* src = tmp + q * 128 + G * 32 + p * 8;
      const v4u v0 = tr_read2(src, src + 512), v1 = tr_read2(src + 1024, src + 1536);
      LAS char* dst = sm + K2_SR + lane * SR_ROW + cw * 32;
      *(LAS v4u*)dst = v0;
      *(LAS v4u*)(dst + 16) = v1;
    }
  }
  WG_BARRIER();
  {
    const int row = tid >> 3, c8 = tid & 7;
    LAS char* rp = sm + K2_RED + row * RED_ROW + c8 * 32;
    f32x4 lo = *(LAS f32x4*)rp, hi = *(LAS f32x4*)(rp + 16);
#pragma unroll
    for (int s = 1; s < 4; ++s) { lo += *(LAS f32x4*)(rp + s * RED_W); hi += *(LAS f32x4*)(rp + s * RED_W + 16); }
    const v4u sv = *(LAS v4u*)(sm + K2_SR + row * SR_ROW + c8 * 16);
    const float ri = __builtin_amdgcn_rcpf(bf_hi(*(LAS unsigned*)(sm + K2_STAT + row * 4))), r2 = ri * ri;
    v4u o;
#pragma unroll
    for (int e = 0; e < 4; ++e) {
      const float b0 = e < 2 ? lo[2 * e] : hi[2 * e - 4], b1 = e < 2 ? lo[2 * e + 1] : hi[2 * e - 3];
      o[e] = pk_bf(__builtin_fmaf(b0, r2, bf_lo(sv[e])), __builtin_fmaf(b1, r2, bf_hi(sv[e])));
    }
    __builtin_amdgcn_raw_buffer_store_b128(o, __builtin_amdgcn_make_buffer_rsrc((void*)a.out, 0, L_ * D_ * 2, 0x00020000), ((lt * 64 + row) * D_ + dt * 64 + c8 * 8) * 2, 0, AUX_SC1);
  }
}

}

extern "C" void kernel_launch(void* const* d_in, const int* in_sizes, int n_in, void* d_out, int out_size, void* d_ws,
                              size_t ws_size, hipStream_t stream) {
  K1Args a1;
  a1.x = (const u16*)d_in[0];
  a1.lAr = (const u16*)d_in[1];
  a1.Ai = (const u16*)d_in[2];
  a1.Cr = (const u16*)d_in[3];
  a1.Ci = (const u16*)d_in[4];
  a1.ldt = (const u16*)d_in[5];
  a1.Wg = (const u16*)d_in[8];
  a1.Wu = (const u16*)d_in[9];
  a1.Wd = (const u16*)d_in[10];
  a1.ws = (char*)d_ws;
  K2Args a2;
  a2.Dsk = (const u16*)d_in[6];
  a2.ws = (char*)d_ws;
  a2.out = (u16*)d_out;
  (void)hipFuncSetAttribute((const void*)k1, hipFuncAttributeMaxDynamicSharedMemorySize, K1_LDS);
  (void)hipFuncSetAttribute((const void*)k2, hipFuncAttributeMaxDynamicSharedMemorySize, K2_LDS);
  k1<<<256, 512, K1_LDS, stream>>>(a1);
  k2<<<256, 512, K2_LDS, stream>>>(a2);
}
```
